```python
import torch

def _softmax(x, axis=-1):
    return torch.softmax(x, dim=axis)
def _gelu(x):
    return torch.nn.functional.gelu(x, approximate="tanh")
def _silu(x):
    return torch.nn.functional.silu(x)
def _sigmoid(x):
    return torch.sigmoid(x)
def _layernorm(x, axis=-1, eps=1e-5):
    m = x.mean(dim=axis, keepdim=True)
    v = x.var(dim=axis, keepdim=True, unbiased=False)
    return (x - m) / torch.sqrt(v + eps)
def _scatter_add(tgt, idx, src, axis=0):
    return tgt.clone().index_add_(axis, idx, src)
def _segment_sum(src, seg, n):
    out = torch.zeros((n,) + src.shape[1:], dtype=src.dtype)
    return out.index_add_(0, seg, src)
def _pad(x, pw):
    flat = tuple(v for pair in reversed(pw) for v in pair)
    return torch.nn.functional.pad(x, flat)

kernel_name = 'gated_mix__linear_kernel_hedgehog__dense_activation_gelu__prompt'

def setup_inputs():

    _w = (lambda a: (lambda t: t.to(torch.bfloat16) if t.is_floating_point() else t)(torch.as_tensor(a)))
    def _A_setup():
        B, S, D, H = 1, 2048, 448, 16
        Dh = D // H
        return {
            "x": _w((lambda *s: torch.randn(*s, dtype=torch.bfloat16) * 0.05)(B, S, D)),
            "wqkv": _w((lambda *s: torch.randn(*s, dtype=torch.bfloat16) * 0.8)(D, 3 * D)),
            "w_phi": _w((lambda *s: torch.randn(*s, dtype=torch.bfloat16) * 0.8)(H, Dh, Dh) * (Dh ** -0.5)),
            "wo": _w((lambda *s: torch.randn(*s, dtype=torch.bfloat16) * 0.05)(D, D)),
        }
    _A_in = _A_setup()

    def _B_setup():
        B, S, D, F = 1, 2048, 448, 1792
        return {
            "x": _w((lambda *s: torch.randn(*s, dtype=torch.bfloat16) * 0.05)(B, S, D)),
            "w1": _w((lambda *s: torch.randn(*s, dtype=torch.bfloat16) * 3.2)(D, F) * (D ** -0.5)),
            "b1": _w((lambda *s: torch.randn(*s, dtype=torch.bfloat16) * 3.2)(F) * 0.01),
            "w2": _w((lambda *s: torch.randn(*s, dtype=torch.bfloat16) * 0.2)(F, D) * (F ** -0.5)),
            "b2": _w((lambda *s: torch.randn(*s, dtype=torch.bfloat16) * 0.4)(D) * 0.01),
        }
    _B_in = _B_setup()

    return {
        "A_x": _A_in["x"],
        "A_wqkv": _A_in["wqkv"],
        "A_w_phi": _A_in["w_phi"],
        "A_wo": _A_in["wo"],
        "B_x": _B_in["x"],
        "B_w1": _B_in["w1"],
        "B_b1": _B_in["b1"],
        "B_w2": _B_in["w2"],
        "B_b2": _B_in["b2"],
        "gate_w": _w((lambda *s: torch.randn(*s, dtype=torch.bfloat16) * 0.05)(448) * 16.0),
    }

def reference(A_x, A_wqkv, A_w_phi, A_wo, B_x, B_w1, B_b1, B_w2, B_b2, gate_w):
    def _A_ref(x, wqkv, w_phi, wo):
        B, S, D = x.shape
        H = 16; Dh = D // H
        qkv = (lambda x, *d: x.permute(*d))((x @ wqkv).reshape(B, S, 3, H, Dh), 2, 0, 3, 1, 4)
        q, k, v = qkv[0], qkv[1], qkv[2]
        pq = torch.einsum("bhsd,hdf->bhsf", q, w_phi)
        pk = torch.einsum("bhsd,hdf->bhsf", k, w_phi)
        phi_q = torch.concatenate([_softmax(pq, axis=-1), _softmax(-pq, axis=-1)], axis=-1)
        phi_k = torch.concatenate([_softmax(pk, axis=-1), _softmax(-pk, axis=-1)], axis=-1)
        kv = torch.einsum("bhsf,bhsd->bhfd", phi_k, v)
        z  = torch.einsum("bhsf->bhf", phi_k)
        num = torch.einsum("bhqf,bhfd->bhqd", phi_q, kv)
        den = torch.einsum("bhqf,bhf->bhq", phi_q, z)[..., None] + 1e-6
        o = num / den
        return (lambda x, *d: x.permute(*d))(o, 0, 2, 1, 3).reshape(B, S, D) @ wo

    def _B_ref(x, w1, b1, w2, b2):
        h = torch.einsum("bsd,df->bsf", x, w1) + b1
        h = _gelu(h)
        return torch.einsum("bsf,fd->bsd", h, w2) + b2

    x = A_x
    g = _sigmoid((x * gate_w).sum(-1, keepdims=True))
    a = _A_ref(x, A_wqkv, A_w_phi, A_wo)
    b = _B_ref(x, B_w1, B_b1, B_w2, B_b2)
    return g * a + (1 - g) * b
```

```cpp
#include <hip/hip_runtime.h>
#include <hip/hip_bf16.h>
typedef unsigned short u16;
__device__ __forceinline__ float bf2f(u16 v){ return __uint_as_float(((unsigned)v)<<16); }
__device__ __forceinline__ u16 f2bf(float f){ __hip_bfloat16 b = __float2bfloat16(f); return *(u16*)&b; }
constexpr int S=2048, D=448, H=16, DH=28, F=1792;

__global__ void gemm_naive(const u16* A, const u16* B, float* C, int M, int N, int K){
  __shared__ float sa[16][17], sb[16][17];
  int tx=threadIdx.x&15, ty=threadIdx.x>>4;
  int r=blockIdx.y*16+ty, c=blockIdx.x*16+tx;
  float acc=0;
  for(int k0=0;k0<K;k0+=16){
    sa[ty][tx]=bf2f(A[r*K+k0+tx]);
    sb[ty][tx]=bf2f(B[(k0+ty)*N+c]);
    __syncthreads();
    for(int k=0;k<16;k++) acc+=sa[ty][k]*sb[k][tx];
    __syncthreads();
  }
  C[r*N+c]=acc;
}
__global__ void phi_kernel(const float* qkv, const u16* wphi, float* phi){
  int idx=blockIdx.x*blockDim.x+threadIdx.x;
  if(idx>=2*S*H) return;
  int h=idx%H, s=(idx/H)%S, w=idx/(H*S);
  const float* q=qkv+s*3*D+w*D+h*DH;
  float* o=phi+(size_t)idx*56;
  float mx=-1e30f, mn=1e30f;
  for(int f=0;f<DH;f++){ float a=0; for(int d=0;d<DH;d++) a+=bf2f(f2bf(q[d]))*bf2f(wphi[(h*DH+d)*DH+f]); o[f]=a; mx=fmaxf(mx,a); mn=fminf(mn,a);}
  float s1=0,s2=0;
  for(int f=0;f<DH;f++){ float a=o[f]; float e1=__expf(a-mx), e2=__expf(mn-a); o[f]=e1; o[28+f]=e2; s1+=e1; s2+=e2;}
  for(int f=0;f<DH;f++){ o[f]/=s1; o[28+f]/=s2; }
}
__global__ void kv_kernel(const float* qkv, const float* phi, float* kv){
  int idx=blockIdx.x*blockDim.x+threadIdx.x;
  if(idx>=H*56*29) return;
  int d=idx%29, f=(idx/29)%56, h=idx/(29*56);
  float a=0;
  for(int s=0;s<S;s++){ float p=phi[((size_t)(S+s)*H+h)*56+f]; float v= d<28? qkv[s*3*D+2*D+h*DH+d]:1.f; a+=p*v; }
  kv[idx]=a;
}
__global__ void o_kernel(const float* phi, const float* kv, u16* o){
  int idx=blockIdx.x*blockDim.x+threadIdx.x;
  if(idx>=S*D) return;
  int c=idx%D, s=idx/D, h=c/DH, d=c%DH;
  float num=0, den=0;
  for(int f=0;f<56;f++){ float p=phi[((size_t)s*H+h)*56+f]; num+=p*kv[(h*56+f)*29+d]; den+=p*kv[(h*56+f)*29+28]; }
  o[idx]=f2bf(num/(den+1e-6f));
}
__global__ void gelu_kernel(const float* hpre, const u16* b1, u16* h){
  int idx=blockIdx.x*blockDim.x+threadIdx.x;
  if(idx>=S*F) return;
  float x=hpre[idx]+bf2f(b1[idx%F]);
  float t=tanhf(0.7978845608f*(x+0.044715f*x*x*x));
  h[idx]=f2bf(0.5f*x*(1.f+t));
}
__global__ void final_kernel(const u16* x, const u16* gw, const float* a, const float* b, const u16* b2, u16* out){
  int s=blockIdx.x; __shared__ float red[64];
  float p=0; for(int c=threadIdx.x;c<D;c+=64) p+=bf2f(x[s*D+c])*bf2f(gw[c]);
  red[threadIdx.x]=p; __syncthreads();
  float t=0; for(int i=0;i<64;i++) t+=red[i];
  float g=1.f/(1.f+__expf(-t));
  for(int c=threadIdx.x;c<D;c+=64) out[s*D+c]=f2bf(g*a[s*D+c]+(1.f-g)*(b[s*D+c]+bf2f(b2[c])));
}
extern "C" void kernel_launch(void* const* d_in, const int* in_sizes, int n_in,
                              void* d_out, int out_size, void* d_ws, size_t ws_size,
                              hipStream_t stream) {
  const u16* x=(const u16*)d_in[0]; const u16* wqkv=(const u16*)d_in[1]; const u16* wphi=(const u16*)d_in[2];
  const u16* wo=(const u16*)d_in[3]; const u16* w1=(const u16*)d_in[5]; const u16* b1=(const u16*)d_in[6];
  const u16* w2=(const u16*)d_in[7]; const u16* b2=(const u16*)d_in[8]; const u16* gw=(const u16*)d_in[9];
  char* ws=(char*)d_ws;
  float* qkv=(float*)ws; ws+=(size_t)S*3*D*4;
  float* hpre=(float*)ws; ws+=(size_t)S*F*4;
  float* phi=(float*)ws; ws+=(size_t)2*S*H*56*4;
  float* kv=(float*)ws; ws+=(size_t)H*56*32*4;
  u16* o=(u16*)ws; ws+=(size_t)S*D*2;
  u16* h=(u16*)ws; ws+=(size_t)S*F*2;
  float* a=(float*)ws; ws+=(size_t)S*D*4;
  float* b=(float*)ws; ws+=(size_t)S*D*4;
  gemm_naive<<<dim3(3*D/16,S/16),256,0,stream>>>(x,wqkv,qkv,S,3*D,D);
  gemm_naive<<<dim3(F/16,S/16),256,0,stream>>>(x,w1,hpre,S,F,D);
  phi_kernel<<<(2*S*H+255)/256,256,0,stream>>>(qkv,wphi,phi);
  kv_kernel<<<(H*56*29+63)/64,64,0,stream>>>(qkv,phi,kv);
  o_kernel<<<(S*D+255)/256,256,0,stream>>>(phi,kv,o);
  gelu_kernel<<<(S*F+255)/256,256,0,stream>>>(hpre,b1,h);
  gemm_naive<<<dim3(D/16,S/16),256,0,stream>>>(o,wo,a,S,D,D);
  gemm_naive<<<dim3(D/16,S/16),256,0,stream>>>(h,w2,b,S,D,F);
  final_kernel<<<S,64,0,stream>>>(x,gw,a,b,b2,(u16*)d_out);
}
```

```cpp
#pragma clang diagnostic ignored "-Wint-to-pointer-cast"
#include <hip/hip_runtime.h>

#if defined(__HIP_DEVICE_COMPILE__) && __HIP_DEVICE_COMPILE__
#define NOPK __attribute__((target("no-packed-fp32-ops")))
#else
#define NOPK
#endif
#define DI __device__ __forceinline__ NOPK
#define LAS __attribute__((address_space(3)))

typedef unsigned u32;
typedef unsigned long long u64;
typedef __bf16 b2 __attribute__((ext_vector_type(2)));
typedef __bf16 b4 __attribute__((ext_vector_type(4)));
typedef __bf16 b8 __attribute__((ext_vector_type(8)));
typedef float f2 __attribute__((ext_vector_type(2)));
typedef float f4 __attribute__((ext_vector_type(4)));
typedef float f16v __attribute__((ext_vector_type(16)));
typedef u32 u2 __attribute__((ext_vector_type(2)));
typedef u32 u4 __attribute__((ext_vector_type(4)));

#define MAPR 4

constexpr u32 WS_KVF = 4096;
constexpr u32 WS_AF = 128 * 1024;
constexpr u32 WS_KVB = 1024 * 1024;
constexpr u32 WS_HB = 2 * 1024 * 1024;
constexpr u32 WS_END = WS_HB + 64 * 72 * 2048;
constexpr u32 WS_STAMP = 16 * 1024 * 1024;

constexpr u32 K = 1024;
constexpr u32 L_LINEA = 0, L_LINEB = 56 * K, L_PHI = 112 * K, L_V = 128 * K, L_WQ = 124 * K;
constexpr u32 L_WPHI = 152 * K, L_GATE = 156 * K, L_B1 = 157 * K, L_DUMMY = 158 * K, L_KVS = 152 * K;
#define slot_x(j) ((j) == 0 ? 0 : (j) == 1 ? 24 * K : (j) == 2 ? 56 * K : (j) == 3 ? 80 * K : (j) == 4 ? 112 * K : (j) == 5 ? 0 : 136 * K)
#define slot_w(j) ((j) == 6 ? 104 * K : slot_x(j) + 16 * K)

constexpr float LOG2E = 1.4426950408889634f;
constexpr int SPIN_CAP = 1 << 15;

#define MFMA(a, b, c) __builtin_amdgcn_mfma_f32_32x32x16_bf16((a), (b), (c), 0, 0, 0)
#define WAITV(n) asm volatile("s_waitcnt vmcnt(%0)" ::"i"(n) : "memory")
#define BAR() asm volatile("s_waitcnt lgkmcnt(0)\n\ts_barrier" ::: "memory")
#define PRIO(n) __builtin_amdgcn_s_setprio(n)

#define STAMP(i)

DI u4 make_srd(const void* p, u32 bytes) {
  const u64 a = (u64)p;
  u4 r = {(u32)a, (u32)(a >> 32) & 0xffffu, bytes, 0x00020000u};
  return r;
}
DI void dma(u4 srd, u32 voff, u32 soff, u32 lds) {
  asm volatile("s_mov_b32 m0, %0\n\ts_nop 0\n\tbuffer_load_dwordx4 %1, %2, %3 offen lds" ::"s"(lds), "v"(voff), "s"(srd), "s"(soff) : "memory");
}
DI void st16_dev(u4 srd, u32 voff, u32 soff, u4 v) {
  asm volatile("buffer_store_dwordx4 %0, %1, %2, %3 offen sc1\n\ts_nop 1" ::"v"(v), "v"(voff), "s"(srd), "s"(soff) : "memory");
}
DI void st8_dev(u4 srd, u32 voff, u32 soff, u2 v) {
  asm volatile("buffer_store_dwordx2 %0, %1, %2, %3 offen sc1" ::"v"(v), "v"(voff), "s"(srd), "s"(soff) : "memory");
}
template <int OFF>
DI void ld16_dev(u4& d, u4 srd, u32 voff, u32 soff) {
  asm volatile("buffer_load_dwordx4 %0, %1, %2, %3 offen offset:%4 sc1" : "=v"(d) : "v"(voff), "s"(srd), "s"(soff), "i"(OFF) : "memory");
}
DI void poll(u4 srd, u32 voff, u2 tag) {
#pragma unroll 1
  for (int spin = 0; spin < SPIN_CAP; ++spin) {
    u2 v;
    asm volatile("buffer_load_dwordx2 %0, %1, %2, 0 offen sc1\n\ts_waitcnt vmcnt(0)" : "=&v"(v) : "v"(voff), "s"(srd) : "memory");
    if (__builtin_amdgcn_ballot_w64(v.x != tag.x || v.y != tag.y) == 0) break;
    __builtin_amdgcn_s_sleep(1);
  }
}
DI b4 trr(u32 a) { return __builtin_amdgcn_ds_read_tr16_b64_v4bf16((LAS b4*)a); }
DI b8 cat(b4 a, b4 b) { return __builtin_shufflevector(a, b, 0, 1, 2, 3, 4, 5, 6, 7); }
DI u4 lds16(u32 a) { return *(LAS u4*)a; }
DI u32 pk(float a, float b) { f2 v = {a, b}; return __builtin_bit_cast(u32, __builtin_convertvector(v, b2)); }
DI float dot2(u32 a, u32 b, float c) { return __builtin_amdgcn_fdot2_f32_bf16(__builtin_bit_cast(b2, a), __builtin_bit_cast(b2, b), c, false); }
DI b8 asb8(u4 v) { return __builtin_bit_cast(b8, v); }
DI u4 pack8(const f16v& a, int s) {
  u4 r = {pk(a[8 * s], a[8 * s + 1]), pk(a[8 * s + 2], a[8 * s + 3]), pk(a[8 * s + 4], a[8 * s + 5]), pk(a[8 * s + 6], a[8 * s + 7])};
  return r;
}
DI float ex2(float x) { return __builtin_amdgcn_exp2f(x); }
DI float rcp(float x) { return __builtin_amdgcn_rcpf(x); }
DI void halves(float v, float& lo, float& hi) {
  const u32 u = __builtin_bit_cast(u32, v);
  auto r = __builtin_amdgcn_permlane32_swap(u, u, false, false);
  lo = __builtin_bit_cast(float, (u32)r[0]);
  hi = __builtin_bit_cast(float, (u32)r[1]);
}
template <int ROWB, int S0, int S1>
DI f16v wgemm(f16v acc, u32 base, const b8 (&xf)[28]) {
#pragma unroll
  for (int s = S0; s < S1; ++s) acc = MFMA(cat(trr(base + s * 16 * ROWB), trr(base + s * 16 * ROWB + 4 * ROWB)), xf[s], acc);
  return acc;
}
DI void feature_exp(const f16v& t, const b8 (&wp)[2], int h, float (&ep)[14], float (&en)[14]) {
  f16v p = {};
  p = MFMA(wp[0], asb8(pack8(t, 0)), p);
  p = MFMA(wp[1], asb8(pack8(t, 1)), p);
  float v[14];
#pragma unroll
  for (int i = 0; i < 12; ++i) v[i] = p[i];
  v[12] = h ? p[14] : p[12];
  v[13] = h ? p[15] : p[13];
  float mx = __builtin_fmaxf(v[0], v[1]), mn = __builtin_fminf(v[0], v[1]);
#pragma unroll
  for (int i = 2; i < 14; i += 2) { mx = __builtin_fmaxf(__builtin_fmaxf(mx, v[i]), v[i + 1]); mn = __builtin_fminf(__builtin_fminf(mn, v[i]), v[i + 1]); }
  float a, b;
  halves(mx, a, b); mx = __builtin_fmaxf(a, b) * -LOG2E;
  halves(mn, a, b); mn = __builtin_fminf(a, b) * LOG2E;
#pragma unroll
  for (int i = 0; i < 14; ++i) { ep[i] = ex2(__builtin_fmaf(v[i], LOG2E, mx)); en[i] = ex2(__builtin_fmaf(v[i], -LOG2E, mn)); }
}
DI void gelu_store(const f16v& a, float sg, u4 srd, u32 voff, u32 soff) {
  constexpr float C1 = -2.f * LOG2E * 0.7978845608028654f, C2 = C1 * 0.044715f;
  f16v y;
#pragma unroll
  for (int i = 0; i < 16; ++i) {
    const float x = a[i];
    const float e = ex2(x * __builtin_fmaf(x * x, C2, C1));
    y[i] = x * rcp(__builtin_fmaf(e, sg, sg));
  }
  st16_dev(srd, voff, soff, pack8(y, 0));
  st16_dev(srd, voff + 1024, soff, pack8(y, 1));
}

__global__ __launch_bounds__(512, 2) NOPK void gated_mix_kernel(const void* __restrict__ px, const void* __restrict__ pwqkv, const void* __restrict__ pwphi,
                                                               const void* __restrict__ pwo, const void* __restrict__ pw1, const void* __restrict__ pb1,
                                                               const void* __restrict__ pw2, const void* __restrict__ pb2, const void* __restrict__ pgw,
                                                               void* __restrict__ pout, void* pws) {
  __shared__ __attribute__((aligned(1024))) char lds_mem[160 * 1024];
  asm volatile("" ::"v"((LAS char*)lds_mem) : "memory");
  STAMP(0);
  asm volatile("" ::"s"(px), "s"(pwqkv), "s"(pwphi), "s"(pwo), "s"(pw1), "s"(pb1), "s"(pw2), "s"(pb2), "s"(pgw), "s"(pout), "s"(pws));
  const int tid = __builtin_amdgcn_workitem_id_x(), bid = __builtin_amdgcn_workgroup_id_x();
  const int w = __builtin_amdgcn_readfirstlane(tid >> 6), l = tid & 63;
  const int rg = w & 3, role = w >> 2;
  const int r = l & 31, h = l >> 5, blk = (l >> 4) & 1, q = (l & 15) >> 2, p = l & 3;
  const int xcd = bid & 7, idx = bid >> 3;
  const int chalf = xcd & 1, rb = (xcd >> 1) * 4 + (idx >> 3);
  const int c = chalf * 8 + (idx & 7), hq = c ^ 1;
  const int sgl = (0x52 >> (2 * (c >> 2))) & 3;
  const bool two = (c & 3) != sgl;
  const int lineA = 7 * (c >> 2) + (c & 3), lineB = 7 * (c >> 2) + 4 + (c & 3) - ((c & 3) > sgl);
  const int heavy = chalf ? 3 : 6;
  const bool has2 = (idx & 7) != heavy;
  const int rt64 = xcd * 4 + (idx >> 3), cg = (idx & 7) - ((idx & 7) > heavy);

  const u4 sx = make_srd(px, 2048 * 896), sqkv = make_srd(pwqkv, 448 * 2688), sw1 = make_srd(pw1, 448 * 3584);
  const u4 sw1b = make_srd(pw1, two ? 448 * 3584 : 0);
  const u4 sw2 = make_srd(pw2, has2 ? 1792 * 896 : 0), swo = make_srd(pwo, 448 * 896), sws = make_srd(pws, WS_END);

  const u32 vx0 = (rb * 128 + 16 * w + (l >> 3)) * 896 + (((l & 7) ^ (l >> 4)) << 4);
  const u32 vx1 = (vx0 ^ 64) + 8 * 896;
  const u32 hcol = p == 3 ? 40 : p * 16;
  const u32 vkv = (16 * rg + (l >> 2)) * 2688 + (1 + role) * 896 + 56 * c + hcol;
  const u32 lswz = ((l & 7) ^ ((l >> 2) & 4)) << 4;
  const u32 vla = (8 * w + (l >> 3)) * 3584 + lineA * 128 + lswz;
  const u32 dB = (lineB - lineA) * 128;
  const u32 wk = w * K;
#define CHUNK(j) do { dma(sx, vx0, (j) * 128, slot_x(j) + 2 * wk); dma(sx, vx1, (j) * 128, slot_x(j) + 2 * wk + K); \
                      dma(sqkv, vkv, (j) * 64 * 2688, slot_w(j) + wk); } while (0)
#define LINEA(i) dma(sw1, vla, (i) * 8 * 3584, L_LINEA + (i) * K + wk)
#define LINEB(i) dma(sw1b, vla, (i) * 8 * 3584 + dB, L_LINEB + (i) * K + wk)
  CHUNK(0);
  {
    u4 sm; u32 vm, lm;
    if (w < 4) { sm = make_srd(pwphi, 16 * 1568); vm = (w < 2 ? c : hq) * 1568 + (w & 1) * K + l * 16; lm = L_WPHI + wk; }
    else if (w == 4) { sm = make_srd(pgw, 896); vm = l * 16; lm = L_GATE; }
    else { sm = make_srd(pb1, w == 5 ? 3584 : 0); vm = ((l & 8) ? lineB : lineA) * 128 + (l & 7) * 16; lm = w == 5 ? L_B1 : L_DUMMY; }
    dma(sm, vm, 0, lm);
  }
  CHUNK(1);
  CHUNK(2);
  u64 e0;
  asm volatile("s_load_dwordx2 %0, %1, 0x0\n\ts_waitcnt lgkmcnt(0)" : "=&s"(e0) : "s"(pws) : "memory");
  const u64 e1 = e0 * 6364136223846793005ull + 1442695040888963407ull;
  const u2 tag = {(u32)e1, (u32)(e1 >> 32)};

  b8 xf[28];
  f16v acc = {};
  float gd0 = 0.f, gd1 = 0.f;
  const u32 xrd = (32 * rg + r) * 128 + ((h ^ ((r >> 1) & 7)) << 4);
  const u32 hrd = (8 * h + q) * 64 + blk * 32 + p * 8;
#pragma unroll
  for (int j = 0; j < 7; ++j) {
    WAITV(6);
    BAR();
    if (j == 0) STAMP(1);
    if (j < 4) CHUNK(j + 3);
    if (j == 4) { LINEA(24); LINEA(32); LINEA(40); }
    if (j == 5) { LINEA(48); LINEB(0); LINEB(8); }
    if (j == 6) { LINEA(0); LINEA(8); LINEA(16); }
#pragma unroll
    for (int s = 0; s < 4; ++s) {
      const u4 xv = lds16((xrd ^ (s << 5)) + slot_x(j));
      const u4 gv = lds16(L_GATE + (4 * j + s) * 32 + h * 16);
      const u32 a = hrd + role * 4096 + slot_w(j) + s * 1024;
      xf[4 * j + s] = asb8(xv);
      acc = MFMA(cat(trr(a), trr(a + 256)), xf[4 * j + s], acc);
      gd0 = dot2(xv.x, gv.x, gd0); gd1 = dot2(xv.y, gv.y, gd1);
      gd0 = dot2(xv.z, gv.z, gd0); gd1 = dot2(xv.w, gv.w, gd1);
    }
  }
  LINEB(16); LINEB(24); LINEB(32); LINEB(40);
  STAMP(2);
  float gt;
  { float a, b; halves(gd0 + gd1, a, b); gt = (a + b) * LOG2E; }
  const float sg = __builtin_fminf(1.f + ex2(gt), 1e30f);
  b8 wp[2];
  {
    const u32 a = L_WPHI + role * 2048 + q * 56 + ((l & 19) == 19 ? 48 : 32 * blk + 8 * p);
    const u32 ah = a + h * 4 * 56;
    b4 t3 = trr(a + 24 * 56);
    u2 z = __builtin_bit_cast(u2, t3);
    z.x = h ? z.x : 0; z.y = h ? z.y : 0;
    wp[0] = cat(trr(ah), trr(ah + 8 * 56));
    wp[1] = cat(trr(ah + 16 * 56), __builtin_bit_cast(b4, z));
  }
  u32 biasA = *(LAS unsigned short*)(L_B1 + (32 * role + r) * 2), biasB = *(LAS unsigned short*)(L_B1 + 128 + (32 * role + r) * 2);
  const u4 one0 = {h ? 0u : 0x3f80u, 0, 0, 0};
  const u32 lrd = (8 * h + q) * 128 + ((role ^ (q >> 1)) << 6) + blk * 32 + p * 8;
  const int tg = rb * 4 + rg;
  const u32 vst = l * 16;
  const u32 sA = WS_HB + (tg * 72 + 2 * lineA + role) * 2048, sB = WS_HB + (tg * 72 + 2 * lineB + role) * 2048;
  const u32 fA = WS_AF + (tg * 72 + 2 * lineA + role) * 128, fB = WS_AF + (tg * 72 + 2 * lineB + role) * 128;
  const u32 vfl = l == 0 ? 0 : 0x7ffffff0;
  const u32 vq = (16 * w + (l >> 2)) * 2688 + 56 * hq + hcol;
#define WQ(i) dma(sqkv, vq, (i) * 16 * 2688, L_WQ + (i) * K + wk)
  const u32 vw2 = (448 * rg + 16 * role + (l >> 3)) * 896 + cg * 128 + lswz;
  const u32 lw2 = rg * 4096 + role * 2048;
#define W2R(i) do { dma(sw2, vw2, (i) * 32 * 896, ((i) % 9) * 16 * K + lw2); dma(sw2, vw2, ((i) * 32 + 8) * 896, ((i) % 9) * 16 * K + lw2 + K); } while (0)
  const f16v zero = {};

  if (role == 0) {
    PRIO(3);
    {
      float ep[14], en[14];
      feature_exp(acc, wp, h, ep, en);
      float sp = ep[0] + ep[1], sn = en[0] + en[1];
#pragma unroll
      for (int i = 2; i < 14; ++i) { sp += ep[i]; sn += en[i]; }
      float a, b;
      halves(sp, a, b); sp = rcp(a + b);
      halves(sn, a, b); sn = rcp(a + b);
      const u32 sig = ((r & 2) << 2) | ((r >> 1) & 6) | (r & 1);
      const u32 wa = L_PHI + (32 * rg + r) * 128 + ((sig ^ h) << 3);
#pragma unroll
      for (int g = 0; g < 4; ++g) {
        u2 vp = {pk(ep[4 * g] * sp, ep[4 * g + 1] * sp), g < 3 ? pk(ep[(4 * g + 2) % 14] * sp, ep[(4 * g + 3) % 14] * sp) : 0u};
        u2 vn = {pk(en[4 * g] * sn, en[4 * g + 1] * sn), g < 3 ? pk(en[(4 * g + 2) % 14] * sn, en[(4 * g + 3) % 14] * sn) : 0u};
        *(LAS u2*)(wa ^ (g << 4)) = vp;
        *(LAS u2*)(wa ^ (g << 4) ^ 64) = vn;
      }
    }
    WAITV(4);
    BAR();
    STAMP(3);
    LINEB(48); WQ(12); WQ(20);
    if (w < 2) {
      f16v kv = {};
      const u32 u = w * 8 + blk * 4 + p;
#pragma unroll
      for (int s = 0; s < 8; ++s) {
        b4 fa[2], fb[2];
#pragma unroll
        for (int t = 0; t < 2; ++t) {
          const u32 tok = 8 * h + 4 * t + q;
          const u32 sig = ((q & 2) << 2) | (h << 2) | (t << 1) | (q & 1);
          fa[t] = trr(L_PHI + s * 2048 + tok * 128 + ((u ^ sig) << 3));
          fb[t] = trr(L_V + s * 1024 + tok * 64 + (((blk * 4 + p) ^ (tok >> 1)) << 3));
        }
        kv = MFMA(cat(fa[0], fa[1]), cat(fb[0], fb[1]), kv);
      }
      const u32 so = WS_KVB + ((c * 16 + rb) * 4 + 2 * w) * K;
      st16_dev(sws, vst, so, pack8(kv, 0));
      st16_dev(sws, vst + 1024, so, pack8(kv, 1));
    }
    PRIO(1);
    { u4 ba = {biasA, 0, 0, 0}; acc = MFMA(asb8(ba), asb8(one0), zero); }
    acc = wgemm<128, 0, 10>(acc, L_LINEA + lrd, xf);
    WAITV(0);
    if (w < 2) st8_dev(sws, vfl, WS_KVF + ((c * 16 + rb) * 2 + w) * 128, tag);
    acc = wgemm<128, 10, 28>(acc, L_LINEA + lrd, xf);
    BAR();
    STAMP(4);
    PRIO(0);
    WQ(0); WQ(8);
    W2R(0); W2R(1); W2R(2);
    gelu_store(acc, sg, sws, vst, sA);
    if (two) {
      PRIO(1);
      { u4 ba = {biasB, 0, 0, 0}; acc = MFMA(asb8(ba), asb8(one0), zero); }
      acc = wgemm<128, 0, 28>(acc, L_LINEB + lrd, xf);
      PRIO(0);
    }
    WAITV(0);
    st8_dev(sws, vfl, fA, tag);
    BAR();
    STAMP(5);
    W2R(3); W2R(4); W2R(5); W2R(6);
    if (two) gelu_store(acc, sg, sws, vst, sB);
    poll(sws, WS_KVF + ((hq * 16 + (l & 15)) * 2 + (w >> 1)) * 128, tag);
    if (two) st8_dev(sws, vfl, fB, tag);
    STAMP(6);
    {
      u4 pf[16];
      const u32 so = WS_KVB + (hq * 64 + w) * K;
#pragma unroll
      for (int i = 0; i < 16; ++i) ld16_dev<0>(pf[i], sws, vst, so + i * 4096);
      u4 id;
#pragma unroll
      for (int i = 0; i < 4; ++i) {
        const int m0 = 8 * (i >> 1) + 4 * h + 2 * (i & 1);
        id[i] = r == m0 ? 0x3f80u : r == m0 + 1 ? 0x3f800000u : 0u;
      }
      f16v sum = {};
#pragma unroll
      for (int i = 0; i < 16; ++i) {
        asm volatile("s_waitcnt vmcnt(%1)" : "+v"(pf[i]) : "i"(15 - i) : "memory");
        sum = MFMA(asb8(id), asb8(pf[i]), sum);
      }
      u4 o = pack8(sum, 0);
#pragma unroll
      for (int i = 0; i < 4; ++i) o[i] = r == 25 ? 0x3f803f80u : o[i];
      *(LAS u4*)(L_KVS + wk + l * 16) = o;
    }
    BAR();
    STAMP(7);
    W2R(7); W2R(8);
  } else {
    {
      const u32 wa = L_V + (32 * rg + r) * 64 + ((((r >> 1) & 7) ^ h) << 3);
#pragma unroll
      for (int g = 0; g < 4; ++g) {
        u2 vv = {pk(acc[4 * g], acc[4 * g + 1]), pk(acc[4 * g + 2], acc[4 * g + 3])};
        if (g == 3) { vv.x = h ? vv.x : 0x3f80u; vv.y = h ? vv.y : 0u; }
        *(LAS u2*)(wa ^ (g << 4)) = vv;
      }
    }
    WAITV(4);
    BAR();
    STAMP(3);
    PRIO(2);
    LINEB(48); WQ(12); WQ(20);
    { u4 ba = {biasA, 0, 0, 0}; acc = MFMA(asb8(ba), asb8(one0), zero); }
    acc = wgemm<128, 0, 28>(acc, L_LINEA + lrd, xf);
    PRIO(0);
    gelu_store(acc, sg, sws, vst, sA);
    WAITV(2);
    BAR();
    STAMP(4);
    PRIO(1);
    WQ(0);
    W2R(0); W2R(1); W2R(2);
    if (two) {
      { u4 ba = {biasB, 0, 0, 0}; acc = MFMA(asb8(ba), asb8(one0), zero); }
      acc = wgemm<128, 0, 28>(acc, L_LINEB + lrd, xf);
    }
    PRIO(0);
    WAITV(6);
    st8_dev(sws, vfl, fA, tag);
    if (two) gelu_store(acc, sg, sws, vst, sB);
    BAR();
    STAMP(5);
    PRIO(1);
    W2R(3); W2R(4); W2R(5); W2R(6);
    acc = wgemm<64, 0, 28>(zero, L_WQ + hrd, xf);
    PRIO(0);
    WAITV(8);
    if (two) st8_dev(sws, vfl, fB, tag);
    float ep[14], en[14];
    feature_exp(acc, wp, h, ep, en);
    u4 fp[2], fn[2];
#pragma unroll
    for (int i = 0; i < 7; ++i) { fp[i >> 2][i & 3] = pk(ep[2 * i], ep[2 * i + 1]); fn[i >> 2][i & 3] = pk(en[2 * i], en[2 * i + 1]); }
    fp[1][3] = 0; fn[1][3] = 0;
    STAMP(6);
    if (two) WAITV(1); else WAITV(0);
    BAR();
    STAMP(7);
    PRIO(3);
    f16v np = MFMA(asb8(lds16(L_KVS + l * 16)), asb8(fp[0]), zero);
    f16v nn = MFMA(asb8(lds16(L_KVS + 2 * K + l * 16)), asb8(fn[0]), zero);
    np = MFMA(asb8(lds16(L_KVS + K + l * 16)), asb8(fp[1]), np);
    nn = MFMA(asb8(lds16(L_KVS + 3 * K + l * 16)), asb8(fn[1]), nn);
    const float rp = rcp(np[13]), rn = rcp(nn[13]);
    const float og = rcp(1.f + ex2(-gt)) * rcp(__builtin_fmaf(np[12], rp, __builtin_fmaf(nn[12], rn, 1e-6f)));
    float al, be, x_;
    halves(rp * og, al, x_);
    halves(rn * og, be, x_);
    f16v o;
#pragma unroll
    for (int i = 0; i < 16; ++i) o[i] = __builtin_fmaf(np[i], al, nn[i] * be);
    u4 o0 = pack8(o, 0), o1 = pack8(o, 1);
    const u32 om = hq == 0 ? pk(rcp(1.f + ex2(gt)), 0.f) : 0u;
    o1.z = h ? o1.z : om;
    o1.w = h ? o1.w : 0u;
    const u32 so = WS_HB + (tg * 72 + 56 + hq) * 2048;
    st16_dev(sws, vst, so, o0);
    st16_dev(sws, vst + 1024, so, o1);
    PRIO(0);
    W2R(7); W2R(8);
    WAITV(4);
    st8_dev(sws, vfl, WS_AF + (tg * 72 + 56 + hq) * 128, tag);
  }
  STAMP(8);
  if (has2) {
    const int tg2 = rt64 * 2 + role;
    const u32 fbase = WS_AF + tg2 * 72 * 128;
    const u32 sh = WS_HB + (tg2 * 72 + 14 * rg) * 2048, sob = WS_HB + (tg2 * 72 + 56 + 4 * rg) * 2048;
    const u32 vwo0 = (112 * rg + 16 * role + (l >> 3)) * 896 + cg * 128 + lswz;
    const u32 vwo1 = (112 * rg + 16 * role + 8 + ((l >> 3) & (role ? 3 : 7))) * 896 + cg * 128 + lswz;
#define WOR(i) do { dma(swo, vwo0, ((i) - 14) * 28 * 896, ((i) % 9) * 16 * K + lw2); dma(swo, vwo1, ((i) - 14) * 28 * 896, ((i) % 9) * 16 * K + lw2 + K); } while (0)
    u4 af[18][2];
#define ALD(i) do { const u32 so_ = (i) < 14 ? sh + (i) * 2048 : sob + ((i) - 14) * 2048; ld16_dev<0>(af[i][0], sws, vst, so_); ld16_dev<1024>(af[i][1], sws, vst, so_); } while (0)
    u32 bias2[2];
    asm volatile("global_load_ushort %0, %2, %3\n\tglobal_load_ushort %1, %2, %3 offset:64" : "=&v"(bias2[0]), "=&v"(bias2[1]) : "v"((cg * 64 + r) * 2), "s"(pb2) : "memory");
    poll(sws, fbase + (14 * rg + (l & 7)) * 128, tag);
    asm volatile("" : "+v"(bias2[0]), "+v"(bias2[1]));
    STAMP(9);
    ALD(0); ALD(1); ALD(2); ALD(3); ALD(4); ALD(5);
    f16v o0 = {}, o1 = {};
    const u32 wrd = rg * 4096 + (4 * h + q) * 128 + ((q >> 1) << 6) + blk * 32 + p * 8;
#define ROUND(i, n) do { asm volatile("s_waitcnt vmcnt(%2)" : "+v"(af[i][0]), "+v"(af[i][1]) : "i"(n) : "memory"); \
      _Pragma("unroll") for (int s = 0; s < 2; ++s) { const u32 a = wrd + ((i) % 9) * 16 * K + s * 2048; \
        u4 w0 = __builtin_bit_cast(u4, cat(trr(a), trr(a + 1024))), w1 = __builtin_bit_cast(u4, cat(trr(a ^ 64), trr((a ^ 64) + 1024))); \
        if ((i) == 14 && s == 1) { w0.z = h ? w0.z : bias2[0]; w1.z = h ? w1.z : bias2[1]; } \
        o0 = MFMA(asb8(af[i][s]), asb8(w0), o0); o1 = MFMA(asb8(af[i][s]), asb8(w1), o1); } } while (0)
    ROUND(0, 10); ROUND(1, 8); ROUND(2, 6);
    BAR();
    poll(sws, fbase + (14 * rg + 8 + (l & 7) % 6) * 128, tag);
    STAMP(10);
    ALD(6); ALD(7); ALD(8);
    W2R(9); W2R(10); W2R(11);
    ROUND(3, 12); ROUND(4, 12); ROUND(5, 12);
    BAR();
    ALD(9); ALD(10); ALD(11);
    W2R(12); W2R(13); WOR(14);
    ROUND(6, 22); ROUND(7, 20); ROUND(8, 18);
    WAITV(12);
    BAR();
    ALD(12); ALD(13);
    WOR(15); WOR(16); WOR(17);
    ROUND(9, 20); ROUND(10, 18); ROUND(11, 16);
    STAMP(11);
    poll(sws, fbase + (56 + 4 * rg + (l & 3)) * 128, tag);
    STAMP(12);
    ALD(14); ALD(15); ALD(16); ALD(17);
    BAR();
    ROUND(12, 8); ROUND(13, 8);
    BAR();
    ROUND(14, 6); ROUND(15, 4); ROUND(16, 2); ROUND(17, 0);
    STAMP(13);
    {
      const u32 wa = w * 10 * K + l * 4;
#pragma unroll
      for (int i = 0; i < 16; ++i) {
        const u32 o = (i >> 2) * 1280 + (i & 1) * 272 + (i & 2) * 320;
        *(LAS float*)(wa + o) = o0[i];
        *(LAS float*)(wa + o + 5120) = o1[i];
      }
    }
    BAR();
    {
      const u32 t = l >> 3;
      const u32 ra = role * 40 * K + ((l >> 2) & 1) * 5120 + rg * 1280 + (t & 1) * 272 + (t & 2) * 320 + h * 128 + p * 32;
      f4 s0 = *(LAS f4*)ra, s1 = *(LAS f4*)(ra + 16);
#pragma unroll
      for (int k = 1; k < 4; ++k) { s0 += *(LAS f4*)(ra + k * 10 * K); s1 += *(LAS f4*)(ra + k * 10 * K + 16); }
      u4 o = {pk(s0.x, s0.y), pk(s0.z, s0.w), pk(s1.x, s1.y), pk(s1.z, s1.w)};
      *(u4*)((char*)pout + (rt64 * 64 + 8 * w + t) * 896 + cg * 128 + (l & 7) * 16) = o;
    }
    if (bid == 0 && w == 0) st8_dev(sws, vfl, 0, tag);
  }
  STAMP(14);
}

extern "C" void kernel_launch(void* const* d_in, const int* in_sizes, int n_in, void* d_out, int out_size, void* d_ws, size_t ws_size,
                              hipStream_t stream) {
  gated_mix_kernel<<<256, 512, 0, stream>>>(d_in[0], d_in[1], d_in[2], d_in[3], d_in[5], d_in[6], d_in[7], d_in[8], d_in[9], d_out, d_ws);
}
```

```cpp
#pragma clang diagnostic ignored "-Wint-to-pointer-cast"
#include <hip/hip_runtime.h>

#define DI __device__ __forceinline__
#define LAS __attribute__((address_space(3)))

typedef unsigned u32;
typedef unsigned long long u64;
typedef __bf16 b2 __attribute__((ext_vector_type(2)));
typedef __bf16 b4 __attribute__((ext_vector_type(4)));
typedef __bf16 b8 __attribute__((ext_vector_type(8)));
typedef float f2 __attribute__((ext_vector_type(2)));
typedef float f4 __attribute__((ext_vector_type(4)));
typedef float f16v __attribute__((ext_vector_type(16)));
typedef u32 u2 __attribute__((ext_vector_type(2)));
typedef u32 u4 __attribute__((ext_vector_type(4)));
typedef u32 u8v __attribute__((ext_vector_type(8)));
typedef u32 u16v __attribute__((ext_vector_type(16)));

#define MAP2 0
#define SA16 1
#define EPF 1
#define XP 0
#define HT 0
#define FLW 16
#define SLP 2
#define MAPR 2

constexpr u32 K = 1024;
constexpr u32 WS_G = 4 * K;
constexpr u32 WS_WF = 32 * K;
constexpr u32 WS_KC = 64 * K;
constexpr u32 WS_TC = 128 * K;
constexpr u32 WS_KVB = 1024 * K;
constexpr u32 WS_PT = 2048 * K;
constexpr u32 WS_HB = 4096 * K;
constexpr u32 WS_END = WS_HB + 32 * 72 * 4096;
constexpr u32 WS_STAMP = 16 * 1024 * 1024;
constexpr u32 S0 = 0, S1 = 32 * K, S2 = 64 * K, S3 = 96 * K, S4 = 128 * K;
constexpr u32 L_PHI = S1, L_V = S1 + 16 * K, L_KVS = S1 + 24 * K, L_SYNC = S1 + 28 * K, L_OMG = S1 + 29 * K;

constexpr float LOG2E = 1.4426950408889634f;
constexpr int SPIN_CAP = 1 << 15;
constexpr u32 OOB = 0x7ffffff0u;

#define MFMA(a, b, c) __builtin_amdgcn_mfma_f32_32x32x16_bf16((a), (b), (c), 0, 0, 0)
#define WAITV(n) asm volatile("s_waitcnt vmcnt(%0)" ::"i"(n) : "memory")
#define WAITR(r, n) asm volatile("s_waitcnt vmcnt(%1)" : "+v"(r) : "i"(n) : "memory")
#define WAITL(n)
#define BAR() do { WAITL(0); asm volatile("s_waitcnt lgkmcnt(0)\n\ts_barrier" ::: "memory"); } while (0)
#define PULLED() asm volatile("s_waitcnt lgkmcnt(0)" ::: "memory")
#define PRIO(n) __builtin_amdgcn_s_setprio(n)
#define PIN() __builtin_amdgcn_sched_barrier(0)
#define KEEP(...) asm volatile("" ::__VA_ARGS__)
#define MS(d, a, b) asm volatile("v_mfma_f32_16x16x32_bf16 %0, %1, %2, %0" : "+v"(d) : "v"(a), "v"(b))
#define MSC(d, a, b, c) asm volatile("v_mfma_f32_16x16x32_bf16 %0, %1, %2, %3" : "=&v"(d) : "v"(a), "v"(b), "v"(c))
#define MS0(d, a, b) asm volatile("v_mfma_f32_16x16x32_bf16 %0, %1, %2, 0" : "=&v"(d) : "v"(a), "v"(b))
#define MF(d, a, b, c) do { PIN(); d = MFMA(a, b, c); KEEP("v"(d)); PIN(); } while (0)
#define M0S(mb, mi, sb, si) asm volatile("s_add_u32 m0, %1, %2\n\ts_add_u32 %0, %3, %4" : "=s"(so) : "s"(mb), "i"(mi), "s"(sb), "i"(si) : "scc", "memory")
#define M0C(mb, mi, i1, i2) asm volatile("s_add_u32 m0, %2, %3\n\ts_mov_b32 %0, %4\n\ts_mov_b32 %1, %5" : "=s"(so), "=s"(so2) : "s"(mb), "i"(mi), "i"(i1), "i"(i2) : "scc", "memory")
#define SETM0(mb, mi) asm volatile("s_add_u32 m0, %0, %1\n\ts_nop 0" ::"s"(mb), "i"(mi) : "scc", "memory")
#define M0SD(mb, mi, sb, si, srd, v, sc) asm volatile("s_add_u32 m0, %1, %2\n\ts_add_u32 %0, %3, %4\n\tbuffer_load_dwordx4 %5, %6, %0 offen" sc " lds" \
  : "=&s"(so) : "s"(mb), "i"(mi), "s"(sb), "i"(si), "v"(v), "s"(srd) : "scc", "memory")
#define M0CD(mb, mi, i1, i2, srd, v) asm volatile("s_add_u32 m0, %2, %3\n\ts_mov_b32 %0, %4\n\tbuffer_load_dwordx4 %6, %7, %0 offen lds\n\ts_mov_b32 %1, %5" \
  : "=&s"(so), "=&s"(so2) : "s"(mb), "i"(mi), "i"(i1), "i"(i2), "v"(v), "s"(srd) : "scc", "memory")
#define DMA(srd, v, s, off) asm volatile("buffer_load_dwordx4 %0, %1, %2 offen offset:%3 lds" ::"v"(v), "s"(srd), "s"(s), "i"(off) : "memory")
#define LD16P(d, srd, v, off) asm volatile("buffer_load_dwordx4 %0, %1, %2, 0 offen offset:%3" : "=v"(d) : "v"(v), "s"(srd), "i"(off) : "memory")
#define LDU16(d, srd, v, off) asm volatile("buffer_load_ushort %0, %1, %2, 0 offen offset:%3" : "+v"(d) : "v"(v), "s"(srd), "i"(off) : "memory")
#define DMAD(srd, v, s, off) asm volatile("buffer_load_dwordx4 %0, %1, %2 offen offset:%3 sc1 lds" ::"v"(v), "s"(srd), "s"(s), "i"(off) : "memory")
#define SCX " sc1"
#define LD16(d, srd, v, s, off) asm volatile("buffer_load_dwordx4 %0, %1, %2, %3 offen offset:%4" SCX : "=v"(d) : "v"(v), "s"(srd), "s"(s), "i"(off) : "memory")
#define LD4(d, srd, v, s) asm volatile("buffer_load_dword %0, %1, %2, %3 offen sc1" : "+v"(d) : "v"(v), "s"(srd), "s"(s) : "memory")
#define ST16(srd, v, s, off, d) asm volatile("buffer_store_dwordx4 %0, %1, %2, %3 offen offset:%4 sc1\n\ts_nop 1" ::"v"(d), "v"((XQ & 4) ? OOB : v), "s"(srd), "s"(s), "i"(off) : "memory")
#define FLAG(v, s, off) asm volatile("buffer_store_dwordx2 %0, %1, %2, %3 offen offset:%4 sc1" ::"v"(tag), "v"((XQ & 2) ? OOB : v), "s"(sws), "s"(s), "i"(off) : "memory")
#define COUNT(v, s, off) asm volatile("buffer_atomic_add_x2 %0, %1, %2, %3 offen offset:%4 sc1" ::"v"(one), "v"(v), "s"(sws), "s"(s), "i"(off) : "memory")
#define ST4(srd, v, s, d) asm volatile("buffer_store_dword %0, %1, %2, %3 offen sc1" ::"v"(d), "v"(v), "s"(srd), "s"(s) : "memory")
#define XQ 0
#define SPQ(d, off) asm volatile("s_load_dwordx2 %0, %1, %2 glc" : "=&s"(d) : "s"(pws), "s"(off) : "memory")
#define SPQC(d, off) asm volatile("s_load_dwordx2 %0, %1, %2" : "=&s"(d) : "s"(pws), "s"(off) : "memory")
#define SPW(...) asm volatile("s_waitcnt lgkmcnt(0)" : __VA_ARGS__::"memory")
#define DMA_A DMA
#define MF_A MF
#define MF_2 MF
#define RD_A
#define ABL0()
#define BAR_A BAR
#define SPC(d, off, want, nsc) do { if (__builtin_expect(d != (want), 0)) spin(sws, pws, off, want, nsc); } while (0)

#define CST(i)

DI void spin(u4 srd, u64 pws, u32 off, u64 want, int nsc) {
#pragma unroll 1
  for (int n = 0; n < SPIN_CAP; ++n) {
    u64 d;
    __builtin_amdgcn_s_sleep(SLP);
    if (n < nsc) asm volatile("s_load_dwordx2 %0, %1, %2 glc\n\ts_waitcnt lgkmcnt(0)" : "=&s"(d) : "s"(pws), "s"(off) : "memory");
    else {
      u2 v;
      asm volatile("buffer_load_dwordx2 %0, off, %1, %2 sc1\n\ts_waitcnt vmcnt(0)" : "=&v"(v) : "s"(srd), "s"(off) : "memory");
      d = (u32)__builtin_amdgcn_readfirstlane(v.x) | (u64)(u32)__builtin_amdgcn_readfirstlane(v.y) << 32;
    }
    if (d == want) break;
  }
}
DI void spin6(u4 srd, u32 voff, u64 want) {
#pragma unroll 1
  for (int n = 0; n < SPIN_CAP; ++n) {
    u64 v;
    asm volatile("buffer_load_dwordx2 %0, %1, %2, 0 offen sc1\n\ts_waitcnt vmcnt(0)" : "=&v"(v) : "v"(voff), "s"(srd) : "memory");
    if ((__builtin_amdgcn_ballot_w64(v != want) & 0x3f) == 0) break;
    __builtin_amdgcn_s_sleep(SLP);
  }
}
DI u32 opaque(u32 v) { asm volatile("" : "+v"(v)); return v; }
DI u32 opaque_s(u32 v) { asm volatile("" : "+s"(v)); return v; }
DI b4 trr(u32 a) { return __builtin_amdgcn_ds_read_tr16_b64_v4bf16((LAS b4*)a); }
DI b8 cat(b4 a, b4 b) { return __builtin_shufflevector(a, b, 0, 1, 2, 3, 4, 5, 6, 7); }
DI u4 lds16(u32 a) { return *(LAS u4*)a; }
DI u32 pk(float a, float b) { f2 v = {a, b}; return __builtin_bit_cast(u32, __builtin_convertvector(v, b2)); }
DI b8 asb8(u4 v) { return __builtin_bit_cast(b8, v); }
DI u4 pack8(const f16v& a, int s) {
  u4 r = {pk(a[8 * s], a[8 * s + 1]), pk(a[8 * s + 2], a[8 * s + 3]), pk(a[8 * s + 4], a[8 * s + 5]), pk(a[8 * s + 6], a[8 * s + 7])};
  return r;
}
DI float ex2(float x) { return __builtin_amdgcn_exp2f(x); }
DI float rcp(float x) { return __builtin_amdgcn_rcpf(x); }
DI float fmx(float a, float b) { return __builtin_elementwise_maximum(a, b); }
DI float fmn(float a, float b) { return __builtin_elementwise_minimum(a, b); }
DI float asf(u32 v) { return __builtin_bit_cast(float, v); }
DI void halves(float v, float& lo, float& hi) {
  const u32 u = __builtin_bit_cast(u32, v);
  auto r = __builtin_amdgcn_permlane32_swap(u, u, false, false);
  lo = asf(r[0]);
  hi = asf(r[1]);
}
#define ST4X(e) do { _Pragma("unroll") for (int i = 0; i < 4; ++i) t_[i] = e; KEEP("v"(t_[0]), "v"(t_[1]), "v"(t_[2]), "v"(t_[3])); PIN(); } while (0)
#define GELU4(a_, g, o, hf) do { const float a[4] = {a_[4 * g], a_[4 * g + 1], a_[4 * g + 2], a_[4 * g + 3]}; constexpr float C1 = -2.f * LOG2E * 0.7978845608028654f, C2 = C1 * 0.044715f; float t_[4]; PIN(); \
  ST4X(a[i] * a[i]); ST4X(__builtin_fmaf(t_[i], C2, C1)); ST4X(a[i] * t_[i]); ST4X(ex2(t_[i])); ST4X(1.f + t_[i]); \
    \
  u32 o0_, o1_; \
  asm volatile("v_rcp_f32 %2, %2\n\tv_rcp_f32 %3, %3\n\tv_rcp_f32 %4, %4\n\tv_rcp_f32 %5, %5\n\t" \
               "v_mul_f32 %2, %6, %2\n\tv_mul_f32 %3, %7, %3\n\tv_mul_f32 %4, %8, %4\n\tv_mul_f32 %5, %9, %5\n\tv_cvt_pk_bf16_f32 %0, %2, %3\n\tv_cvt_pk_bf16_f32 %1, %4, %5" \
               : "=&v"(o0_), "=v"(o1_), "+v"(t_[0]), "+v"(t_[1]), "+v"(t_[2]), "+v"(t_[3]) : "v"(a[0]), "v"(a[1]), "v"(a[2]), "v"(a[3])); \
  o[2 * (hf)] = o0_; o[2 * (hf) + 1] = o1_; PIN(); } while (0)
#define MM0(a) do { PIN(); mx = fmx(a[0], a[1]); mn = fmn(a[0], a[1]); KEEP("v"(mx), "v"(mn)); PIN(); } while (0)
#define MM1(a, i0, i1) do { PIN(); _Pragma("unroll") for (int i = i0; i < i1; i += 2) { mx = fmx(fmx(mx, a[i]), a[i + 1]); mn = fmn(fmn(mn, a[i]), a[i + 1]); } \
  KEEP("v"(mx), "v"(mn)); PIN(); } while (0)
#define MM2() do { PIN(); float a_, b_; halves(mx, a_, b_); mx = fmx(a_, b_) * -LOG2E; halves(mn, a_, b_); mn = fmn(a_, b_) * LOG2E; KEEP("v"(mx), "v"(mn)); PIN(); } while (0)
#define EXPS(a, i0, i1) do { PIN(); _Pragma("unroll") for (int i = i0; i < i1; ++i) { \
  ep[i] = ex2(__builtin_fmaf(a[i], LOG2E, mx)); en[i] = ex2(__builtin_fmaf(a[i], -LOG2E, mn)); } \
  _Pragma("unroll") for (int i = i0; i < i1; ++i) KEEP("v"(ep[i]), "v"(en[i])); PIN(); } while (0)
#define PULL1(d, o, ct) d[ct] = cat(trr(rb##ct + (o)), trr(rb##ct + (o) + (SA16 ? 2048 : 1024)))
#define PULLX(d, ct) do { const u32 a_ = opaque(rb##ct - (S3 - S1) - m4); d[ct] = cat(trr(a_), trr(a_ + 2048)); } while (0)
#define PULL(d, o) do { PULL1(d, o, 0); PULL1(d, o, 1); PULL1(d, o, 2); PULL1(d, o, 3); PIN(); } while (0)
#define DUMP1(a, i, o) asm volatile("ds_write_addtid_b32 %0 offset:%1" ::"v"(a[i]), "i"((o) + (i) * 256) : "memory")
#define DUMP2(a, da, o) asm volatile("ds_write2st64_b32 %0, %1, %2 offset0:%5 offset1:%6\n\tds_write2st64_b32 %0, %3, %4 offset0:%7 offset1:%8" \
  ::"v"(da), "v"(a[0]), "v"(a[1]), "v"(a[2]), "v"(a[3]), "i"(o), "i"((o) + 1), "i"((o) + 2), "i"((o) + 3) : "memory")
#define DUMP4(a, o) DUMP1(a, 0, o); DUMP1(a, 1, o); DUMP1(a, 2, o); DUMP1(a, 3, o)
#define DUMP16(a, o) do { DUMP4(a[0], o); DUMP4(a[1], (o) + K); DUMP4(a[2], (o) + 2 * K); DUMP4(a[3], (o) + 3 * K); } while (0)

struct Desc { const void* p; u32 bytes, fmt; };
struct Args { Desc x, w1, ws, b1, wqkv, wphi, gw, w2, wo, b2, out; };

__global__ __launch_bounds__(512, 2) void gated_mix_kernel(const Args A) {
  __shared__ __attribute__((aligned(1024))) char lds_mem[160 * 1024];
  asm volatile("" ::"v"((LAS char*)lds_mem) : "memory");
  CST(0);
  u16v ka, kb; u8v kc; u4 sout; u64 pc;
  asm volatile("s_load_dwordx16 %0, %5, 0x0\n\ts_load_dwordx16 %1, %5, 0x40\n\ts_load_dwordx8 %2, %5, 0x80\n\ts_load_dwordx4 %3, %5, 0xa0\n\ts_getpc_b64 %4\n.Lgm_start:"
               : "=&s"(ka), "=&s"(kb), "=&s"(kc), "=&s"(sout), "=&s"(pc) : "s"(__builtin_amdgcn_kernarg_segment_ptr()));
#define SUB4(v, i) __builtin_shufflevector(v, v, 4 * i, 4 * i + 1, 4 * i + 2, 4 * i + 3)
#define ARGS() PIN(); asm volatile("s_waitcnt lgkmcnt(0)" : "+s"(ka), "+s"(kb), "+s"(kc), "+s"(sout) CPA); \
    const u4 sx = SUB4(ka, 0), sws = SUB4(ka, 2), sb1 = SUB4(ka, 3), sqkv = SUB4(kb, 0), sphi = SUB4(kb, 1), sgw = SUB4(kb, 2); \
    const u4 sw2 = SUB4(kb, 3), swo = SUB4(kc, 0), sb2 = SUB4(kc, 1); \
    const u64 pws = ka[8] | (u64)ka[9] << 32
  const int w0 = __builtin_amdgcn_readfirstlane(__builtin_amdgcn_workitem_id_x() >> 6);
  u32 cpf;
  asm volatile("s_and_b32 %0, %3, 5\n\ts_cbranch_scc1 .Lgm_nopf\n\ts_lshr_b32 %0, %2, 3\n\ts_lshl2_add_u32 %0, %0, %3\n\ts_lshl_b32 %0, %0, 8\n\t"
               "s_min_u32 m0, %0, .Lgm_end-.Lgm_start+2304-384\n\t"
               "s_load_dword %0, %1, m0 offset:0x0\n\ts_load_dword %0, %1, m0 offset:0x40\n\ts_load_dword %0, %1, m0 offset:0x80\n\ts_load_dword %0, %1, m0 offset:0xc0\n\t"
               "s_load_dword %0, %1, m0 offset:0x100\n\ts_load_dword %0, %1, m0 offset:0x140\n\ts_load_dword %0, %1, m0 offset:0x180\n\ts_load_dword %0, %1, m0 offset:0x1c0\n.Lgm_nopf:"
               : "=&s"(cpf) : "s"(pc), "s"(__builtin_amdgcn_workgroup_id_x()), "s"(w0) : "scc");
#define CPA , "+s"(cpf)
#define MAP() const int rb = (xcd >> 2) * 8 + (idx >> 2), c = (xcd & 3) * 4 + (idx & 3)
#define IDS() const int bid = opaque_s(__builtin_amdgcn_workgroup_id_x()), w = opaque_s(w0), l = opaque(__builtin_amdgcn_workitem_id_x()) & 63; \
    const int rg = w & 3, r = l & 31, h = l >> 5, blk = (l >> 4) & 1, g4 = l >> 4, q = (l & 15) >> 2, p = l & 3, xcd = bid & 7, idx = bid >> 3; \
    MAP(); \
    const bool two = (c & 3) != 3, has2 = MAP2 ? !((xcd & 1) && (idx & 3) == 2) : (idx & 7) != 6, idle = !two && (rg & 1); \
    const int lineA = 7 * (c >> 2) + (c & 3), rt64 = MAP2 ? (xcd >> 1) * 8 + (idx >> 2) : xcd * 4 + (idx >> 3), ci = MAP2 ? (xcd & 1) * 4 + (idx & 3) : idx & 7, cg = ci == 7 ? 6 : ci
  const f16v zero = {}; const f4 zero4 = {};
#define ENTRY() u64 e0; asm volatile("s_load_dwordx2 %0, %1, 0x0" : "=&s"(e0) : "s"(pws) : "memory")
#define CPT
#define LOADER_ADDR() const u32 mrg = opaque_s(rg * 8192); \
    const u32 vx0 = opaque((rb * 128 + 32 * rg + (l >> 3)) * 896 + (((l & 7) ^ (l >> 4)) << 4)), vx1 = opaque((vx0 ^ 64) + 8 * 896 - 1024); \
      \
    const u32 vla = opaque((16 * rg + (l >> 3)) * 3584 + (((l & 7) ^ ((l >> 2) & 4)) << 4) + lineA * 128 + 1024), vlb = opaque(vla - 512); \
    const u32 vla1 = SA16 ? opaque(vla ^ 32) : vla, vlb1 = SA16 ? opaque(vlb ^ 32) : vlb; \
    ARGS(); \
    u4 sw1 = SUB4(ka, 1); \
    { const u64 a = (sw1.x | (u64)sw1.y << 32) - 3072; sw1.x = (u32)a; sw1.y = (u32)(a >> 32); }
#define DECLS() IDS(); u32 so, so2, sa; u64 e1, pv; u2 one = {1, 0}; u32 sv; b8 wr0[4], wr1[4], wr2[4], wr3[4], wr4[4], wr5[4], wr6[4], wr7[4]; f4 oo[4][4]; \
    u4 af0[4], af1[4], af2[4], af3[4], af4[4], af5[4], af6[4], af7[4], af8[4], af9[4], af10[4], af11[4], af12[4], af13[4]

#define PRE_SEC(slot, sec) M0S(mrg, S3 + (slot), sq0, (sec) * 896); DMA(sqkv, vq0, so, 0); DMA(sqkv, vq1, so, 1024); DMA(sqkv, vq2, so, 2048); DMA(sqkv, vq3, so, 3072)
#define PRE_ADDR() u4 pg0, pg1; asm volatile("" : "=v"(pg0), "=v"(pg1)); const int hd = (c & 12) + (rg & 2); \
    const u32 vq0 = opaque((l >> 3) * 2688 + ((l >> 2) & 1) * 56 + p * 16), sq0 = opaque_s(32 * rb * 2688 + 56 * hd), sph = opaque_s(hd * 1568)
#define PRE_LOAD() if (idle && rb < 14) { PRIO(3); \
      M0S(mrg, S4 + 4096, sph, 0); DMA(sphi, vst, so, 0); DMA(sphi, vst, so, 1024); DMA(sphi, vst, so, 2048); DMA(sphi, vst, so, 3072); \
      const u32 vq1 = vq0 + 8 * 2688 - 1024, vq2 = vq0 + 16 * 2688 - 2048, vq3 = vq0 + 24 * 2688 - 3072, vpg = (r & 29) == 28 ? 64 * rb + 16 * h : OOB; \
      PRE_SEC(0, 1); PRE_SEC(4096, 2); PRE_SEC(32 * K, 0); \
      asm volatile("buffer_load_dwordx4 %0, %2, %3, 0 offen\n\tbuffer_load_dwordx4 %1, %2, %3, 0 offen offset:32" : "+v"(pg0), "+v"(pg1) : "v"(vpg), "s"(sgw) : "memory"); }
#define PRE_PREP() u4 i0, i1; { const u32 d = r - 8 * h, one_ = 0x3f80u << (16 * (d & 1)), e = d >> 1; \
      _Pragma("unroll") for (int i = 0; i < 4; ++i) { i0[i] = e == i ? one_ : 0u; i1[i] = e == 8 + i ? one_ : 0u; } } \
    const u32 pra = opaque(S4 + mrg + 4096 + (8 * h + q) * 56 + ((l & 19) == 19 ? 48 : 32 * blk + 8 * p)); \
    const u32 prw = opaque(S3 + mrg + ((r & 19) | ((r & 4) << 1) | ((r & 8) >> 1)) * 128 + h * 16); \
    const u32 vv = opaque((h * 32 + (r >= 26 && r < 30 ? r ^ 6 : r)) * 16), ss = opaque_s(WS_PT + (((c & 12) + (rg & 2)) * 84 + 2 * rb) * K); \
    asm volatile("" : "+v"(i0), "+v"(i1))
#define PRE_PHI() b8 pw[3][2][2], pp[2][2]; \
    _Pragma("unroll") for (int j = 0; j < 2; ++j) { u2 z = __builtin_bit_cast(u2, trr(pra + j * 1568 + 20 * 56)); z.x = h ? 0 : z.x; z.y = h ? 0 : z.y; \
      pp[j][0] = cat(trr(pra + j * 1568), trr(pra + j * 1568 + 4 * 56)); pp[j][1] = cat(trr(pra + j * 1568 + 16 * 56), __builtin_bit_cast(b4, z)); }
#define PRE_READ(n) _Pragma("unroll") for (int i = 0; i < 4; ++i) pw[n][i >> 1][i & 1] = asb8(lds16(prw + (n < 2 ? n * 4096 : 32 * K) + i * 32))
#define PRE_JOB(n, sec) do { f16v d_[2]; \
    _Pragma("unroll") for (int j = 0; j < 2; ++j) d_[j] = MFMA(pw[n][j][0], sec == 2 ? asb8(i0) : pp[j][0], zero); \
    _Pragma("unroll") for (int j = 0; j < 2; ++j) d_[j] = MFMA(pw[n][j][1], sec == 2 ? asb8(i1) : pp[j][1], d_[j]); \
    _Pragma("unroll") for (int j = 0; j < 2; ++j) { u4 t0 = pack8(d_[j], 0), t1 = pack8(d_[j], 1); \
      asm volatile("" : "+v"(t0), "+v"(t1)); \
      _Pragma("unroll") for (int i = 0; i < 4; ++i) { t0[i] = r < 28 ? t0[i] : sec ? 0u : pg0[i]; t1[i] = r < 28 ? t1[i] : sec ? 0u : pg1[i]; } \
      const u32 s_ = ss + (j * 84 + sec * 28) * K; ST16(sws, vv, s_, 0, t0); ST16(sws, vv, s_, 1024, t1); } } while (0)
#define SU_X() \
  u32 xr0 = (role ? mrg : mrg & 16384) + blk * 4096 + (l & 15) * 128 + ((h ^ ((r >> 1) & 7)) << 4), xr1 = opaque(xr0 ^ 32), xr2 = opaque(xr0 ^ 64), xr3 = opaque(xr0 ^ 96); \
  xr0 = opaque(xr0); \
  asm volatile("" : "+v"(one))
#define SU_W() u32 wd0 = opaque(h * 4096 + 2048 + (rg & 1) * 1024 + q * 128 + ((q >> 1) << 6) + blk * 32 + p * 8), wd1 = opaque(wd0 ^ 64)
#define SU_W16() u32 xs0 = opaque((mrg & 16384) + (l & 15) * 128 + ((g4 ^ ((l >> 1) & 7)) << 4)), xs1 = opaque(xs0 ^ 64); \
  u32 wd0 = opaque(g4 * 4096 + 2048 + (rg & 1) * 1024 + q * 128 + ((q >> 1) << 6) + blk * 32 + p * 8), wd1 = opaque(wd0 ^ 32), wd2 = opaque(wd0 ^ 64), wd3 = opaque(wd0 ^ 96)
#define SU_1() const u32 vcol = opaque(cg * 128 + (l & 7) * 16), vw0 = opaque(has2 ? (l >> 3) * 896 + (vcol ^ (g4 << 5)) : OOB);     \
  const u32 vw1 = opaque(vw0 + 6144), vw2 = opaque(vw0 + 2 * 6144), vw3 = opaque(vw0 + 3 * 6144), swt = opaque_s(14 * rg * 32 * 896)
#define SU_2() const u32 f0 = opaque_s(WS_WF + ((c >> 1) * 16 + ((7 * rg) >> 1)) * 128), f1 = opaque_s(f0 + 128), f2 = opaque_s(f0 + 256), f3 = opaque_s(f0 + 384); \
  const u32 f4_ = opaque_s(WS_WF + ((c >> 1) * 16 + 15) * 128), f5 = opaque_s(WS_WF + ((c >> 2) * 32 + rb) * 128); u64 p0, p1, p2, p3, p4, p5; \
  const u32 sp7 = opaque_s(WS_PT + (c * 84 + 7 * rg) * K), m7 = opaque_s(7 * rg * K)
#define WFQ() do { SPQC(p0, f0); SPQC(p1, f1); SPQC(p2, f2); SPQC(p3, f3); SPQC(p4, f4_); SPQC(p5, f5); } while (0)
#define WFC() do { SPW("+s"(p0), "+s"(p1), "+s"(p2), "+s"(p3), "+s"(p4), "+s"(p5)); \
    if (__builtin_expect(((p0 ^ e1) | (p1 ^ e1) | (p2 ^ e1) | (p3 ^ e1) | (p4 ^ e1) | (p5 ^ e1)) != 0, 0)) { \
      spin6(sws, l < 4 ? f0 + l * 128 : l == 4 ? f4_ : l == 5 ? f5 : OOB, e1); } } while (0)
#define SU_3() const int tg = rb * 4 + rg; \
  const u32 cT = opaque_s(WS_TC + ((rb * 4 + (c >> 2)) * 2 + (rg >> 1)) * 1536), sT = opaque_s(WS_HB + ((tg >> 1) * 144 + (role ? (tg & 1) + 112 + 2 * c : 4 * lineA + 16 * (rg & 1))) * 2048), vst2 = opaque(has2 ? vst : OOB)
#define SU_4() const u32 rb0 = opaque((role ? (rg & 2 ? S0 : S2) : S3 + mrg) + ((SA16 && !role ? 8 : 16) * h + 4 * blk + q) * 128 + ((2 * blk + (q >> 1)) << 5) + p * 8); \
  const u32 rb1 = opaque(rb0 ^ 32), rb2 = opaque(rb0 ^ 64), rb3 = opaque(rb0 ^ 96); \
  const u32 sh = opaque_s(WS_HB + (rt64 * 72 + (role ? 56 + 4 * (rg & 2) : 14 * rg)) * 4096 + role * (rg & 1) * 2048)
#define SU_5() const u32 cP = opaque_s(WS_TC + (((rt64 >> 1) * 4 + (role ? rg & 2 : rg)) * 2 + (rt64 & 1)) * 1536 + role * (4 + ci) * 128); u32 cq; \
  const u32 syw = opaque(L_SYNC + w * 4), syr = opaque(L_SYNC + (l & 7) * 4)
#define TCQ(k) do { cq = cP + (k) * 128; SPQ(pv, cq); } while (0)
#define TCC(n) do { SPW("+s"(pv)); if (has2) SPC(pv, cq, e1 + (n), 64); } while (0)
#define SU_R0() const u32 vfl = opaque(l == 0 && !idle ? 0 : OOB); asm volatile("" : "+v"(one))
#define SU_R1A() const u32 vflk = opaque(l < 16 ? l * 256 : OOB), vflo = opaque(l < 8 ? l * 128 : OOB), ckv = opaque_s(WS_KC + c * 4 * K + (w & 1) * 128); \
  const u32 phw = opaque(L_PHI + (32 * rg + r) * 128 + (((((r & 2) << 2) | ((r >> 1) & 6) | (r & 1)) ^ h) << 3)), fbk = opaque(S1 + vst); \
  const u32 vmw = opaque(L_V + (32 * rg + r) * 64 + ((((r >> 1) & 7) ^ h) << 3))
#define SU_R1B() const u32 pa0_ = opaque(L_PHI + (8 * h + q) * 128 + ((((w & 1) * 8 + blk * 4 + p) ^ (((q & 2) << 2) | (h << 2) | (q & 1))) << 3)); \
    const u32 pb0_ = opaque(L_V + (8 * h + q) * 64 + (((blk * 4 + p) ^ (4 * h + (q >> 1))) << 3)); \
    const u32 pa[2] = {pa0_, opaque(pa0_ ^ 16)}, pb[2] = {pb0_, opaque(pb0_ ^ 16)}; \
    const u32 sk0 = opaque_s(WS_KVB + ((c * 4 + 2 * (w & 1)) * 16 + rb) * K), sk1 = opaque_s(sk0 + 16 * K)
#define SU_R1C() const u32 ckp = opaque_s(WS_KC + ((c * 16 + rb) * 2 + (rg >> 1)) * 128), skv = opaque_s(WS_KVB + (c * 4 + rg) * 16 * K); \
  const u32 so7 = opaque_s(56 * rg * 896)
#define SU_R1E() const u32 rc0 = opaque(h ? (rg & 2 ? S0 : S2) + (24 + 2 * blk + (q & 1)) * 128 + (blk << 5) + p * 8 : rb0 + 1024); \
  const u32 rc1 = opaque(rc0 ^ 32), rc2 = opaque(rc0 ^ 64), rc3 = opaque(rc0 ^ 96), vso = opaque(blk * 1024 + (h * 16 + (l & 15)) * 16)
#define PULLO(d, j, ct, cx) d[ct] = cat(trr(rb##cx + (j) * 3584), trr(rc##cx + (j) * 3584))
#define ARRIVE(n) do { asm volatile("" ::: "memory"); *(volatile LAS u32*)syw = n; } while (0)
#define PEEK() sv = *(volatile LAS u32*)syr
#define MEET(n, m) do { _Pragma("unroll 1") for (int spin = 0; spin < (SPIN_CAP << 5); ++spin) { \
      if ((__builtin_amdgcn_ballot_w64(sv < n) & m) == 0) break; PEEK(); } asm volatile("" ::: "memory"); } while (0)
#define TAG() do { asm volatile("" : "+s"(e0) CPT); e1 = e0 * 6364136223846793005ull + 1442695040888963407ull; asm volatile("" : "+s"(e1)); } while (0)
#define INITC() do { const u4 t4 = {(u32)e1, (u32)(e1 >> 32), (u32)e1, (u32)(e1 >> 32)}; \
    if (rg == 1) { const u32 s_ = WS_TC + (rb * 4 + (c >> 2)) * 3 * K; ST16(sws, vst, s_, 0, t4); ST16(sws, vst, s_, 1024, t4); ST16(sws, vst, s_, 2048, t4); } \
    if (rb == 15) _Pragma("unroll") for (int j = 0; j < 2; ++j) { const u32 s_ = WS_KC + ((c & 12) + (rg & 2) + j) * 4 * K; \
      ST16(sws, vst, s_, 0, t4); ST16(sws, vst, s_, 1024, t4); ST16(sws, vst, s_, 2048, t4); ST16(sws, vst, s_, 3072, t4); } } while (0)
#define WARM(srd, n, first) do { const u32 v_ = l < (n) ? ((first) + bid * (n) + l) * 128 : OOB; \
    asm volatile("buffer_load_dword %0, %1, 0 offen lds" ::"v"(v_), "s"(srd) : "memory"); } while (0)
#define WARMX2(k0) do { const u32 v_ = (l & 7) >= (k0) && (l & 7) < 7 ? ((xcd * 32 + idx) * 8 + (l >> 3)) * 896 + (l & 7) * 128 : OOB; \
    asm volatile("buffer_load_dword %0, %1, 0 offen lds" ::"v"(v_), "s"(sx) : "memory"); } while (0)
#define WARMW(k0) do { const u32 v_ = (l >> 3) >= (k0) && (l >> 3) < 7 && (l & 7) < 7 ? ((l >> 3) * 64 + (xcd >> 2) * 32 + idx) * 3584 + ((xcd & 3) * 7 + (l & 7)) * 128 : OOB; \
    asm volatile("buffer_load_dword %0, %1, 0 offen lds" ::"v"(v_), "s"(SUB4(ka, 1)) : "memory"); } while (0)
#define WARMX(k0) do { const u32 v_ = (l & 7) >= (k0) && (l & 7) < 7 ? (bid * 8 + (l >> 3)) * 896 + (l & 7) * 128 : OOB; \
    asm volatile("buffer_load_dword %0, %1, 0 offen lds" ::"v"(v_), "s"(sx) : "memory"); } while (0)
#define L2W(i) do { const int n_ = idx * 8 + rg * 2 + (i); const u4 s_ = n_ < 196 ? sw2 : swo; const u32 v_ = ((n_ < 196 ? n_ : n_ - 196) * 64 + l) * 128; \
    asm volatile("buffer_load_dword %0, %1, %2, 0 offen" : "=v"(l2w[i]) : "v"(v_), "s"(s_) : "memory"); } while (0)
#define WGFLAG(so_) do { const u2 tag = {(u32)e1, (u32)(e1 >> 32)}; const u32 v_ = l < 16 ? l * 8 : OOB, s_ = so_; FLAG(v_, s_, 0); } while (0)
#define FLIPX() do { xr0 = opaque(xr0 ^ 0x10000); xr1 = opaque(xr1 ^ 0x10000); xr2 = opaque(xr2 ^ 0x10000); xr3 = opaque(xr3 ^ 0x10000); } while (0)
#define FLIP(a) a = opaque(a ^ 0x10000)
#define BIAS(t) do { const u4 one0 = {h == (t >> 1) ? 0x3f80u : 0u, 0, 0, 0}, ba = {bias, 0, 0, 0}; MF(h##t, asb8(ba), asb8(one0), zero); } while (0)
#define BIAS16(t) do { const u4 one0 = {g4 == t ? 0x3f80u : 0u, 0, 0, 0}, ba = {bia, 0, 0, 0}; hb[t] = __builtin_amdgcn_mfma_f32_16x16x32_bf16(asb8(ba), asb8(one0), zero4, 0, 0, 0); KEEP("v"(hb[t])); } while (0)
#define SUMS(i0, i1) do { PIN(); _Pragma("unroll") for (int i = i0; i < i1; ++i) { sp += ep[i]; sn += en[i]; } KEEP("v"(sp), "v"(sn)); PIN(); } while (0)
#define NORM() do { float a, b; halves(sp, a, b); sp = rcp(a + b); halves(sn, a, b); sn = rcp(a + b); KEEP("v"(sp), "v"(sn)); PIN(); } while (0)
#define PHIK(e, sc, g, n) do { PIN(); \
      u2 vp = {pk(e[4 * g] * sc, e[4 * g + 1] * sc), g < 3 ? pk(e[(4 * g + 2) % 14] * sc, e[(4 * g + 3) % 14] * sc) : 0u}; \
      *(LAS u2*)(phw ^ (g << 4) ^ (n << 6)) = vp; PIN(); } while (0)
#define VIMG() do { \
    _Pragma("unroll") for (int g = 0; g < 4; ++g) { u2 vv = {pk(acv[4 * g], acv[4 * g + 1]), g < 3 ? pk(acv[4 * g + 2], acv[4 * g + 3]) : 0x3f80u}; \
      *(LAS u2*)(vmw ^ (g << 4)) = vv; } } while (0)
#define KVPART() do { f16v kv = {}; b8 fa[3], fb[3]; \
    _Pragma("unroll") for (int s = 0; s < 11; ++s) { if (s >= 3) MF(kv, fa[s % 3], fb[s % 3], kv); \
      if (s < 8) { fa[s % 3] = cat(trr(pa[0] + s * 2048), trr(pa[1] + s * 2048 + 512)); fb[s % 3] = cat(trr(pb[0] + s * 1024), trr(pb[1] + s * 1024 + 256)); } } \
    const u4 k0 = pack8(kv, 0), k1 = pack8(kv, 1); \
    ST16(sws, vst, sk0, 0, k0); ST16(sws, vst, sk1, 0, k1); } while (0)
#define PHIQ() u4 fp[2], fn[2]; \
    _Pragma("unroll") for (int i = 0; i < 7; ++i) { fp[i >> 2][i & 3] = pk(ep[2 * i], ep[2 * i + 1]); fn[i >> 2][i & 3] = pk(en[2 * i], en[2 * i + 1]); } \
    fp[1][3] = 0; fn[1][3] = 0; \
    float et = ex2(__builtin_amdgcn_fmed3f(acq[14] * LOG2E, -80.f, 80.f)), omg = rcp(1.f + et), gl; \
    asm volatile("" : "+v"(fp[0]), "+v"(fp[1]), "+v"(fn[0]), "+v"(fn[1]), "+v"(et), "+v"(omg))
#define IDENT() u4 id; { const u32 one = 0x3f80u << (16 * (r & 1)), e = (u32)(r - 4 * h) >> 1; \
      id[0] = e == 0 ? one : 0u; id[1] = e == 1 ? one : 0u; id[2] = e == 4 ? one : 0u; id[3] = e == 5 ? one : 0u; } \
    asm volatile("" : "+v"(id))
#define SU_R1D() const u32 vg = opaque(l < (c == 0 ? 32 : 0) ? l * 4 : OOB), sG = opaque_s(WS_G + tg * 128), vgl = opaque(rg == 0 && has2 ? l * 4 : OOB), sG2 = opaque_s(WS_G + rt64 * 256)
#define KVSHARE() do { u4 o = pack8(sum, 0); \
    _Pragma("unroll") for (int i = 0; i < 4; ++i) o[i] = (r & 27) == 27 ? 0x3f803f80u : o[i]; \
    *(LAS u4*)(L_KVS + rg * K + vst) = o; } while (0)
#define OCOMP() u4 oo0, oo1; { const u32 kb = opaque(L_KVS + l * 16); \
    f16v np = MFMA(asb8(lds16(kb)), asb8(fp[0]), zero), nn = MFMA(asb8(lds16(kb + 2 * K)), asb8(fn[0]), zero); \
    np = MFMA(asb8(lds16(kb + K)), asb8(fp[1]), np); nn = MFMA(asb8(lds16(kb + 3 * K)), asb8(fn[1]), nn); \
    const float rp = rcp(np[15]), rn = rcp(nn[15]); \
    const float og = et * rcp(__builtin_fmaf(np[14], rp, __builtin_fmaf(nn[14], rn, 1e-6f))), al = rp * og, be = rn * og; \
    f16v o; _Pragma("unroll") for (int i = 0; i < 14; ++i) o[i] = __builtin_fmaf(np[i], al, nn[i] * be); \
    o[14] = 0.f; o[15] = 0.f; oo0 = pack8(o, 0); oo1 = pack8(o, 1); }
#define FIN2_PREP() f4 sm[2][2]; const u32 tok = 16 * (rg >> 1) + 4 * ((l >> 3) & 3) + 2 * (rg & 1) + h; const bool sw = l & 2; \
    const u32 fa = opaque(S2 + ((rg >> 1) * 4 + ((l >> 1) & 3)) * K + (2 * (rg & 1) + h) * 256 + (4 * ((l >> 3) & 3) + 2 * (l & 1) + ((l >> 1) & 1)) * 16), fa1 = opaque(fa ^ 16); \
    const u32 ra = opaque(fa + 32 * K), ra1 = opaque(fa1 + 32 * K), ga = opaque(L_OMG + tok * 4), vo = opaque((rt64 * 64 + tok) * 896 + vcol); \
    { const u32 b0 = sw ? bb.z : bb.x, b1 = sw ? bb.w : bb.y, b2_ = sw ? bb.x : bb.z, b3 = sw ? bb.y : bb.w; \
      sm[0][0].x = asf(b0 << 16); sm[0][0].y = asf(b0 & 0xffff0000u); sm[0][0].z = asf(b1 << 16); sm[0][0].w = asf(b1 & 0xffff0000u); \
      sm[0][1].x = asf(b2_ << 16); sm[0][1].y = asf(b2_ & 0xffff0000u); sm[0][1].z = asf(b3 << 16); sm[0][1].w = asf(b3 & 0xffff0000u); } \
    asm volatile("" : "+v"(sm[0][0]), "+v"(sm[0][1])); sm[1][0] = sm[0][0]; sm[1][1] = sm[0][1]
#define FIN2_MLP() do { f4 t_[16]; \
    _Pragma("unroll") for (int k = 0; k < 16; ++k) t_[k] = *(LAS f4*)((k & 1 ? ra1 : ra) + (k & 2 ? 0 : 32 * K) + (k >> 2) * 8192); \
    PIN(); _Pragma("unroll") for (int k = 0; k < 16; ++k) sm[(k >> 1) & 1][k & 1] += t_[k]; \
    asm volatile("" : "+v"(sm[0][0]), "+v"(sm[0][1]), "+v"(sm[1][0]), "+v"(sm[1][1])); } while (0)
#define FINAL2() do { f4 t_[8]; float gm[2]; const u32 s32 = 32 * 896; \
    _Pragma("unroll") for (int k = 0; k < 8; ++k) { if ((k & 3) == 0) gm[k >> 2] = *(LAS float*)(ga + (k >> 2) * 128); \
      t_[k] = *(LAS f4*)((k & 1 ? fa1 : fa) + (k >> 2) * 8192 + ((k >> 1) & 1) * 16384); } \
    PIN(); \
    _Pragma("unroll") for (int t = 0; t < 2; ++t) { const f4 a_ = (sm[t][0] + t_[4 * t] + t_[4 * t + 2]) * gm[t], b_ = (sm[t][1] + t_[4 * t + 1] + t_[4 * t + 3]) * gm[t]; \
      const u32 p0_ = pk(a_.x, a_.y), p1_ = pk(a_.z, a_.w), p2_ = pk(b_.x, b_.y), p3_ = pk(b_.z, b_.w); \
      const u4 o = {sw ? p2_ : p0_, sw ? p3_ : p1_, sw ? p0_ : p2_, sw ? p1_ : p3_}; \
      asm volatile("buffer_store_dwordx4 %0, %1, %2, %3 offen sc1\n\ts_nop 1" ::"v"(o), "v"(vo), "s"(sout), "s"(t * s32) : "memory"); PIN(); } } while (0)
#define FIN_PREP() f4 s0, s1; const u32 tok = 32 * role + 16 * (rg >> 1) + 4 * ((l >> 3) & 3) + 2 * (rg & 1) + h; const bool sw = l & 2; \
    const u32 fl_ = ((rg >> 1) * 4 + ((l >> 1) & 3)) * K + (2 * (rg & 1) + h) * 256 + (4 * ((l >> 3) & 3) + 2 * (l & 1) + ((l >> 1) & 1)) * 16; \
    const u32 ra = opaque((role ? S3 : S4) + fl_), ra1 = opaque(ra ^ 16), fa = opaque(S2 + role * 8192 + fl_), fa1 = opaque(fa ^ 16); \
    const u32 ga = opaque(L_OMG + tok * 4), vo = opaque(has2 ? (rt64 * 64 + tok) * 896 + vcol : OOB); \
    { const u32 b0 = sw ? bb.z : bb.x, b1 = sw ? bb.w : bb.y, b2_ = sw ? bb.x : bb.z, b3 = sw ? bb.y : bb.w; \
      s0.x = asf(b0 << 16); s0.y = asf(b0 & 0xffff0000u); s0.z = asf(b1 << 16); s0.w = asf(b1 & 0xffff0000u); \
      s1.x = asf(b2_ << 16); s1.y = asf(b2_ & 0xffff0000u); s1.z = asf(b3 << 16); s1.w = asf(b3 & 0xffff0000u); } \
    asm volatile("" : "+v"(s0), "+v"(s1))
#define FINAL() do { f4 t_[12]; \
    _Pragma("unroll") for (int k = 0; k < 12; ++k) t_[k] = *(LAS f4*)(k < 8 ? (k & 1 ? ra1 : ra) + (k >> 1) * 8192 : (k & 1 ? fa1 : fa) + ((k >> 1) & 1) * 16384); \
    const float gm = *(LAS float*)ga; PIN(); \
    _Pragma("unroll") for (int k = 0; k < 12; k += 2) { s0 += t_[k]; s1 += t_[k + 1]; } \
    s0 *= gm; s1 *= gm; const u32 p0_ = pk(s0.x, s0.y), p1_ = pk(s0.z, s0.w), p2_ = pk(s1.x, s1.y), p3_ = pk(s1.z, s1.w); \
    const u4 o = {sw ? p2_ : p0_, sw ? p3_ : p1_, sw ? p0_ : p2_, sw ? p1_ : p3_}; \
    asm volatile("buffer_store_dwordx4 %0, %1, %2, 0 offen sc1" ::"v"(o), "v"(vo), "s"(sout) : "memory"); } while (0)

#define DIAG()
#define DUMPC(a, da, o) _Pragma("unroll") for (int i_ = 0; i_ < 16; ++i_) *(LAS float*)(da + (o) + i_ * 256) = a[i_ >> 2][i_ & 3]

  if (w0 >= 4) {
    constexpr int role = 1;
    DECLS();
    b8 xf[28], f_[4]; f16v acc, acv, acq; float ep[14], en[14], mx, mn;
    PRIO(0);
    LOADER_ADDR();
    PRIO(0);
    CST(1);
    M0CD(mrg, 0, 0, 0, sx, vx0);
    DMA_A(sx, vx1, so, 1024);
    DMA_A(sw1, vla, so2, 2048);
    if (two) DMA_A(sw1, vlb, so2, 3072);
    M0CD(mrg, 4096, 14336, 28672, sx, vx0);
    DMA_A(sx, vx1, so, 1024);
    DMA_A(sw1, vla1, so2, 2048);
    if (two) DMA_A(sw1, vlb1, so2, 3072);
    ENTRY();
    const u32 vst = opaque(l * 16);
    SU_X();
    M0CD(mrg, 32768, 128, 229376, sx, vx0);
    DMA_A(sx, vx1, so, 1024);
    DMA_A(sw1, vla, so2, 2048);
    if (two) DMA_A(sw1, vlb, so2, 3072);
    M0CD(mrg, 36864, 14464, 258048, sx, vx0);
    DMA_A(sx, vx1, so, 1024);
    DMA_A(sw1, vla1, so2, 2048);
    if (two) DMA_A(sw1, vlb1, so2, 3072);
    CST(2);
    CST(40);
    if (two) WAITV(8); else WAITV(6);
    CST(54);
    BAR_A();
    CST(3);
    M0CD(mrg, 65536, 256, 458752, sx, vx0);
    DMA_A(sx, vx1, so, 1024);
    DMA_A(sw1, vla, so2, 2048);
    if (two) DMA_A(sw1, vlb, so2, 3072);
    xf[0] = asb8(lds16(xr0 + 0));
    xf[1] = asb8(lds16(xr1 + 0));
    xf[2] = asb8(lds16(xr2 + 0));
    xf[3] = asb8(lds16(xr3 + 0));
    TAG();
    SU_1();
    M0CD(mrg, 69632, 14592, 487424, sx, vx0);
    DMA_A(sx, vx1, so, 1024);
    DMA_A(sw1, vla1, so2, 2048);
    if (two) DMA_A(sw1, vlb1, so2, 3072);
    CST(41);
    if (two) WAITV(8); else WAITV(6);
    CST(55);
    BAR_A();
    CST(4);
    M0CD(mrg, 0, 384, 688128, sx, vx0);
    DMA_A(sx, vx1, so, 1024);
    DMA_A(sw1, vla, so2, 2048);
    if (two) DMA_A(sw1, vlb, so2, 3072);
    xf[4] = asb8(lds16(xr0 + 32768));
    xf[5] = asb8(lds16(xr1 + 32768));
    xf[6] = asb8(lds16(xr2 + 32768));
    xf[7] = asb8(lds16(xr3 + 32768));
    SU_2();
    SU_3();
    M0CD(mrg, 4096, 14720, 716800, sx, vx0);
    DMA_A(sx, vx1, so, 1024);
    DMA_A(sw1, vla1, so2, 2048);
    if (two) DMA_A(sw1, vlb1, so2, 3072);
    CST(42);
    if (two) WAITV(8); else WAITV(6);
    CST(56);
    BAR_A();
    CST(5);
    M0CD(mrg, 32768, 512, 917504, sx, vx0);
    DMA_A(sx, vx1, so, 1024);
    DMA_A(sw1, vla, so2, 2048);
    if (two) DMA_A(sw1, vlb, so2, 3072);
    FLIPX();
    xf[8] = asb8(lds16(xr0 + 0));
    xf[9] = asb8(lds16(xr1 + 0));
    xf[10] = asb8(lds16(xr2 + 0));
    xf[11] = asb8(lds16(xr3 + 0));
    SU_4();
    SU_5();
    M0CD(mrg, 36864, 14848, 946176, sx, vx0);
    DMA_A(sx, vx1, so, 1024);
    DMA_A(sw1, vla1, so2, 2048);
    if (two) DMA_A(sw1, vlb1, so2, 3072);
    M0SD(mrg, 98304, swt, 0, sw2, vw0, "");
    DMA(sw2, vw1, so, 1024);
    DMA(sw2, vw2, so, 2048);
    DMA(sw2, vw3, so, 3072);
    CST(43);
    if (two) WAITV(12); else WAITV(10);
    CST(57);
    BAR_A();
    CST(6);
    M0CD(mrg, 65536, 640, 1146880, sx, vx0);
    DMA_A(sx, vx1, so, 1024);
    DMA_A(sw1, vla, so2, 2048);
    if (two) DMA_A(sw1, vlb, so2, 3072);
    FLIPX();
    xf[12] = asb8(lds16(xr0 + 0));
    xf[13] = asb8(lds16(xr1 + 0));
    xf[14] = asb8(lds16(xr2 + 0));
    xf[15] = asb8(lds16(xr3 + 0));
    SU_R1A();
    M0CD(mrg, 69632, 14976, 1175552, sx, vx0);
    DMA_A(sx, vx1, so, 1024);
    DMA_A(sw1, vla1, so2, 2048);
    if (two) DMA_A(sw1, vlb1, so2, 3072);
    M0SD(mrg, 102400, swt, 57344, sw2, vw0, "");
    DMA(sw2, vw1, so, 1024);
    DMA(sw2, vw2, so, 2048);
    DMA(sw2, vw3, so, 3072);
    CST(44);
    if (two) WAITV(12); else WAITV(10);
    CST(58);
    BAR_A();
    CST(7);
    M0CD(mrg, 0, 768, 1376256, sx, vx0);
    DMA_A(sx, vx1, so, 1024);
    DMA_A(sw1, vla, so2, 2048);
    if (two) DMA_A(sw1, vlb, so2, 3072);
    xf[16] = asb8(lds16(xr0 + 32768));
    xf[17] = asb8(lds16(xr1 + 32768));
    xf[18] = asb8(lds16(xr2 + 32768));
    xf[19] = asb8(lds16(xr3 + 32768));
    SU_R1B();
    WFQ();
    M0CD(mrg, 4096, 15104, 1404928, sx, vx0);
    DMA_A(sx, vx1, so, 1024);
    DMA_A(sw1, vla1, so2, 2048);
    if (two) DMA_A(sw1, vlb1, so2, 3072);
    M0SD(mrg, 131072, swt, 114688, sw2, vw0, "");
    DMA(sw2, vw1, so, 1024);
    DMA(sw2, vw2, so, 2048);
    DMA(sw2, vw3, so, 3072);
    CST(45);
    if (two) WAITV(12); else WAITV(10);
    CST(59);
    BAR_A();
    CST(8);
    WFC();
    M0SD(m7, 32768, sp7, 28672, sws, vst, " sc1");
    DMAD(sws, vst, so, 1024);
    DMAD(sws, vst, so, 2048);
    FLIPX();
    xf[20] = asb8(lds16(xr0 + 0));
    xf[21] = asb8(lds16(xr1 + 0));
    xf[22] = asb8(lds16(xr2 + 0));
    xf[23] = asb8(lds16(xr3 + 0));
    SU_R1C();
    DMAD(sws, vst, so, 3072);
    M0SD(m7, 36864, sp7, 32768, sws, vst, " sc1");
    DMAD(sws, vst, so, 1024);
    DMAD(sws, vst, so, 2048);
    M0SD(mrg, 135168, swt, 172032, sw2, vw0, "");
    DMA(sw2, vw1, so, 1024);
    DMA(sw2, vw2, so, 2048);
    DMA(sw2, vw3, so, 3072);
    CST(46);
    WAITV(11);
    CST(60);
    BAR_A();
    CST(9);
    M0SD(m7, 65536, sp7, 57344, sws, vst, " sc1");
    DMAD(sws, vst, so, 1024);
    DMAD(sws, vst, so, 2048);
    DMAD(sws, vst, so, 3072);
    FLIPX();
    xf[24] = asb8(lds16(xr0 + 0));
    xf[25] = asb8(lds16(xr1 + 0));
    xf[26] = asb8(lds16(xr2 + 0));
    xf[27] = asb8(lds16(xr3 + 0));
    *(volatile LAS u32*)syr = 0;
    M0SD(m7, 69632, sp7, 61440, sws, vst, " sc1");
    DMAD(sws, vst, so, 1024);
    DMAD(sws, vst, so, 2048);
    CST(47);
    WAITV(7);
    CST(61);
    BAR_A();
    PRIO(1);
    CST(10);
    f_[0] = asb8(lds16(fbk + 0));
    f_[1] = asb8(lds16(fbk + 1024));
    f_[2] = asb8(lds16(fbk + 2048));
    f_[3] = asb8(lds16(fbk + 3072));
    MF(acc, f_[0], xf[0], zero);
    f_[0] = asb8(lds16(fbk + 4096));
    M0SD(m7, 0, sp7, 0, sws, vst, " sc1");
    MF(acc, f_[1], xf[1], acc);
    f_[1] = asb8(lds16(fbk + 5120));
    MF(acc, f_[2], xf[2], acc);
    f_[2] = asb8(lds16(fbk + 6144));
    DMAD(sws, vst, so, 1024);
    MF(acc, f_[3], xf[3], acc);
    f_[3] = asb8(lds16(fbk + 7168));
    MF(acc, f_[0], xf[4], acc);
    f_[0] = asb8(lds16(fbk + 8192));
    DMAD(sws, vst, so, 2048);
    MF(acc, f_[1], xf[5], acc);
    f_[1] = asb8(lds16(fbk + 9216));
    MF(acc, f_[2], xf[6], acc);
    f_[2] = asb8(lds16(fbk + 10240));
    DMAD(sws, vst, so, 3072);
    MF(acc, f_[3], xf[7], acc);
    f_[3] = asb8(lds16(fbk + 11264));
    MF(acc, f_[0], xf[8], acc);
    f_[0] = asb8(lds16(fbk + 12288));
    M0SD(m7, 4096, sp7, 4096, sws, vst, " sc1");
    MF(acc, f_[1], xf[9], acc);
    f_[1] = asb8(lds16(fbk + 13312));
    MF(acc, f_[2], xf[10], acc);
    f_[2] = asb8(lds16(fbk + 14336));
    DMAD(sws, vst, so, 1024);
    MF(acc, f_[3], xf[11], acc);
    f_[3] = asb8(lds16(fbk + 15360));
    MF(acc, f_[0], xf[12], acc);
    f_[0] = asb8(lds16(fbk + 16384));
    DMAD(sws, vst, so, 2048);
    MF(acc, f_[1], xf[13], acc);
    f_[1] = asb8(lds16(fbk + 17408));
    MF(acc, f_[2], xf[14], acc);
    f_[2] = asb8(lds16(fbk + 18432));
    WAITV(7);
    ARRIVE(1);
    MF(acc, f_[3], xf[15], acc);
    f_[3] = asb8(lds16(fbk + 19456));
    MF(acc, f_[0], xf[16], acc);
    f_[0] = asb8(lds16(fbk + 20480));
    MF(acc, f_[1], xf[17], acc);
    f_[1] = asb8(lds16(fbk + 21504));
    MF(acc, f_[2], xf[18], acc);
    f_[2] = asb8(lds16(fbk + 22528));
    MF(acc, f_[3], xf[19], acc);
    f_[3] = asb8(lds16(fbk + 23552));
    MF(acc, f_[0], xf[20], acc);
    f_[0] = asb8(lds16(fbk + 24576));
    MF(acc, f_[1], xf[21], acc);
    f_[1] = asb8(lds16(fbk + 25600));
    PEEK();
    MF(acc, f_[2], xf[22], acc);
    f_[2] = asb8(lds16(fbk + 26624));
    MF(acc, f_[3], xf[23], acc);
    f_[3] = asb8(lds16(fbk + 27648));
    MF(acc, f_[0], xf[24], acc);
    MEET(1, 0xf0);
    f_[0] = asb8(lds16(fbk + 32768));
    MF(acc, f_[1], xf[25], acc);
    f_[1] = asb8(lds16(fbk + 33792));
    MF(acc, f_[2], xf[26], acc);
    f_[2] = asb8(lds16(fbk + 34816));
    MF(acc, f_[3], xf[27], acc);
    f_[3] = asb8(lds16(fbk + 35840));
    CST(14);
    MF(acv, f_[0], xf[0], zero);
    f_[0] = asb8(lds16(fbk + 36864));
    MF(acv, f_[1], xf[1], acv);
    f_[1] = asb8(lds16(fbk + 37888));
    MF(acv, f_[2], xf[2], acv);
    f_[2] = asb8(lds16(fbk + 38912));
    MM0(acc);
    MM1(acc, 2, 6);
    MF(acv, f_[3], xf[3], acv);
    f_[3] = asb8(lds16(fbk + 39936));
    MM1(acc, 6, 14);
    MF(acv, f_[0], xf[4], acv);
    f_[0] = asb8(lds16(fbk + 40960));
    MM2();
    MF(acv, f_[1], xf[5], acv);
    f_[1] = asb8(lds16(fbk + 41984));
    MF(acv, f_[2], xf[6], acv);
    f_[2] = asb8(lds16(fbk + 43008));
    EXPS(acc, 0, 1);
    MF(acv, f_[3], xf[7], acv);
    f_[3] = asb8(lds16(fbk + 44032));
    EXPS(acc, 1, 2);
    MF(acv, f_[0], xf[8], acv);
    f_[0] = asb8(lds16(fbk + 45056));
    MF(acv, f_[1], xf[9], acv);
    f_[1] = asb8(lds16(fbk + 46080));
    EXPS(acc, 2, 3);
    MF(acv, f_[2], xf[10], acv);
    f_[2] = asb8(lds16(fbk + 47104));
    EXPS(acc, 3, 4);
    MF(acv, f_[3], xf[11], acv);
    f_[3] = asb8(lds16(fbk + 48128));
    MF(acv, f_[0], xf[12], acv);
    f_[0] = asb8(lds16(fbk + 49152));
    EXPS(acc, 4, 5);
    MF(acv, f_[1], xf[13], acv);
    f_[1] = asb8(lds16(fbk + 50176));
    EXPS(acc, 5, 6);
    MF(acv, f_[2], xf[14], acv);
    f_[2] = asb8(lds16(fbk + 51200));
    WAITV(0);
    ARRIVE(2);
    MF(acv, f_[3], xf[15], acv);
    f_[3] = asb8(lds16(fbk + 52224));
    EXPS(acc, 6, 7);
    MF(acv, f_[0], xf[16], acv);
    f_[0] = asb8(lds16(fbk + 53248));
    EXPS(acc, 7, 8);
    MF(acv, f_[1], xf[17], acv);
    f_[1] = asb8(lds16(fbk + 54272));
    MF(acv, f_[2], xf[18], acv);
    f_[2] = asb8(lds16(fbk + 55296));
    EXPS(acc, 8, 9);
    MF(acv, f_[3], xf[19], acv);
    f_[3] = asb8(lds16(fbk + 56320));
    EXPS(acc, 9, 10);
    MF(acv, f_[0], xf[20], acv);
    f_[0] = asb8(lds16(fbk + 57344));
    MF(acv, f_[1], xf[21], acv);
    f_[1] = asb8(lds16(fbk + 58368));
    EXPS(acc, 10, 11);
    MF(acv, f_[2], xf[22], acv);
    f_[2] = asb8(lds16(fbk + 59392));
    EXPS(acc, 11, 12);
    MF(acv, f_[3], xf[23], acv);
    f_[3] = asb8(lds16(fbk + 60416));
    MF(acv, f_[0], xf[24], acv);
    PEEK();
    EXPS(acc, 12, 13);
    MF(acv, f_[1], xf[25], acv);
    EXPS(acc, 13, 14);
    MF(acv, f_[2], xf[26], acv);
    MF(acv, f_[3], xf[27], acv);
    CST(15);
    PRIO(1);
    float sp = ep[0], sn = en[0];
    SUMS(1, 14);
    NORM();
    MEET(2, 0xf0);
    VIMG();
    PHIK(ep, sp, 0, 0);
    PHIK(en, sn, 0, 1);
    PHIK(ep, sp, 1, 0);
    PHIK(en, sn, 1, 1);
    PHIK(ep, sp, 2, 0);
    PHIK(en, sn, 2, 1);
    PHIK(ep, sp, 3, 0);
    PHIK(en, sn, 3, 1);
    ARRIVE(3);
    PEEK();
    CST(16);
    MEET(3, 0xf0);
    CST(17);
    PRIO(1);
    if (rg < 2) KVPART();
    ARRIVE(4);
    CST(18);
    f_[0] = asb8(lds16(vst + 0));
    f_[1] = asb8(lds16(vst + 1024));
    f_[2] = asb8(lds16(vst + 2048));
    f_[3] = asb8(lds16(vst + 3072));
    MF(acq, f_[0], xf[0], zero);
    f_[0] = asb8(lds16(vst + 4096));
    MF(acq, f_[1], xf[1], acq);
    f_[1] = asb8(lds16(vst + 5120));
    MF(acq, f_[2], xf[2], acq);
    f_[2] = asb8(lds16(vst + 6144));
    M0SD(m7, 65536, so7, 0, swo, vw0, "");
    MF(acq, f_[3], xf[3], acq);
    f_[3] = asb8(lds16(vst + 7168));
    MF(acq, f_[0], xf[4], acq);
    f_[0] = asb8(lds16(vst + 8192));
    MF(acq, f_[1], xf[5], acq);
    f_[1] = asb8(lds16(vst + 9216));
    DMA(swo, vw1, so, 1024);
    MF(acq, f_[2], xf[6], acq);
    f_[2] = asb8(lds16(vst + 10240));
    MF(acq, f_[3], xf[7], acq);
    f_[3] = asb8(lds16(vst + 11264));
    MF(acq, f_[0], xf[8], acq);
    f_[0] = asb8(lds16(vst + 12288));
    DMA(swo, vw2, so, 2048);
    MF(acq, f_[1], xf[9], acq);
    f_[1] = asb8(lds16(vst + 13312));
    MF(acq, f_[2], xf[10], acq);
    f_[2] = asb8(lds16(vst + 14336));
    MF(acq, f_[3], xf[11], acq);
    f_[3] = asb8(lds16(vst + 15360));
    DMA(swo, vw3, so, 3072);
    MF(acq, f_[0], xf[12], acq);
    f_[0] = asb8(lds16(vst + 16384));
    MF(acq, f_[1], xf[13], acq);
    f_[1] = asb8(lds16(vst + 17408));
    MF(acq, f_[2], xf[14], acq);
    f_[2] = asb8(lds16(vst + 18432));
    M0SD(m7, 69632, so7, 28672, swo, vw0, "");
    MF(acq, f_[3], xf[15], acq);
    f_[3] = asb8(lds16(vst + 19456));
    MF(acq, f_[0], xf[16], acq);
    f_[0] = asb8(lds16(vst + 20480));
    MF(acq, f_[1], xf[17], acq);
    f_[1] = asb8(lds16(vst + 21504));
    DMA(swo, vw1, so, 1024);
    MF(acq, f_[2], xf[18], acq);
    f_[2] = asb8(lds16(vst + 22528));
    MF(acq, f_[3], xf[19], acq);
    f_[3] = asb8(lds16(vst + 23552));
    MF(acq, f_[0], xf[20], acq);
    f_[0] = asb8(lds16(vst + 24576));
    DMA(swo, vw2, so, 2048);
    MF(acq, f_[1], xf[21], acq);
    f_[1] = asb8(lds16(vst + 25600));
    MF(acq, f_[2], xf[22], acq);
    f_[2] = asb8(lds16(vst + 26624));
    MF(acq, f_[3], xf[23], acq);
    f_[3] = asb8(lds16(vst + 27648));
    MF(acq, f_[0], xf[24], acq);
    CST(29);
    if (rg < 2) { WAITV(7); COUNT(vflk, ckv, 0); }
    CST(30);
    MF(acq, f_[1], xf[25], acq);
    MF(acq, f_[2], xf[26], acq);
    MF(acq, f_[3], xf[27], acq);
    ARRIVE(5);
    CST(19);
    PRIO(0);
    MM0(acq);
    MM1(acq, 2, 14);
    MM2();
    EXPS(acq, 0, 14);
    PEEK();
    PHIQ();
    IDENT();
    SU_R1D();
    SU_R1E();
    PRIO(3);
    MEET(5, 0xf0);
    M0SD(m7, 0, so7, 200704, swo, vw0, "");
    DMA(swo, vw1, so, 1024);
    DMA(swo, vw2, so, 2048);
    DMA(swo, vw3, so, 3072);
    M0SD(m7, 4096, so7, 229376, swo, vw0, "");
    DMA(swo, vw1, so, 1024);
    DMA(swo, vw2, so, 2048);
    PRIO(3);
    CST(20);
    SPQ(pv, ckp);
    SPW("+s"(pv));
    SPC(pv, ckp, e1 + 16, 64);
    CST(21);
    u4 pf[16];
    sa = skv + 0;
    LD16(pf[0], sws, vst, sa, 0);
    LD16(pf[1], sws, vst, sa, 1024);
    LD16(pf[2], sws, vst, sa, 2048);
    LD16(pf[3], sws, vst, sa, 3072);
    sa = skv + 4096;
    LD16(pf[4], sws, vst, sa, 0);
    LD16(pf[5], sws, vst, sa, 1024);
    LD16(pf[6], sws, vst, sa, 2048);
    LD16(pf[7], sws, vst, sa, 3072);
    sa = skv + 8192;
    LD16(pf[8], sws, vst, sa, 0);
    LD16(pf[9], sws, vst, sa, 1024);
    LD16(pf[10], sws, vst, sa, 2048);
    LD16(pf[11], sws, vst, sa, 3072);
    sa = skv + 12288;
    LD16(pf[12], sws, vst, sa, 0);
    LD16(pf[13], sws, vst, sa, 1024);
    LD16(pf[14], sws, vst, sa, 2048);
    LD16(pf[15], sws, vst, sa, 3072);
    f16v sum;
    WAITR(pf[0], 15);
    MF(sum, asb8(id), asb8(pf[0]), zero);
    WAITR(pf[1], 14);
    MF(sum, asb8(id), asb8(pf[1]), sum);
    WAITR(pf[2], 13);
    MF(sum, asb8(id), asb8(pf[2]), sum);
    WAITR(pf[3], 12);
    MF(sum, asb8(id), asb8(pf[3]), sum);
    WAITR(pf[4], 11);
    MF(sum, asb8(id), asb8(pf[4]), sum);
    WAITR(pf[5], 10);
    MF(sum, asb8(id), asb8(pf[5]), sum);
    WAITR(pf[6], 9);
    MF(sum, asb8(id), asb8(pf[6]), sum);
    WAITR(pf[7], 8);
    MF(sum, asb8(id), asb8(pf[7]), sum);
    WAITR(pf[8], 7);
    MF(sum, asb8(id), asb8(pf[8]), sum);
    WAITR(pf[9], 6);
    MF(sum, asb8(id), asb8(pf[9]), sum);
    WAITR(pf[10], 5);
    MF(sum, asb8(id), asb8(pf[10]), sum);
    WAITR(pf[11], 4);
    MF(sum, asb8(id), asb8(pf[11]), sum);
    WAITR(pf[12], 3);
    MF(sum, asb8(id), asb8(pf[12]), sum);
    WAITR(pf[13], 2);
    MF(sum, asb8(id), asb8(pf[13]), sum);
    WAITR(pf[14], 1);
    MF(sum, asb8(id), asb8(pf[14]), sum);
    WAITR(pf[15], 0);
    MF(sum, asb8(id), asb8(pf[15]), sum);
    CST(22);
    KVSHARE();
    ARRIVE(6);
    PEEK();
    MEET(6, 0xf0);
    CST(23);
    OCOMP();
    if (c == 0) ST4(sws, vg, sG, omg);
    ST16(sws, vso, sT, 0, oo0);
    ST16(sws, vso, sT, 512, oo1);
    CST(24);
    u4 bb; LD16P(bb, sb2, vcol, 0);
    PULLO(wr0, 0, 0, 0); PULLO(wr0, 0, 1, 1); PULLO(wr0, 0, 2, 2); PULLO(wr0, 0, 3, 3); PIN();
    PULLO(wr1, 1, 0, 2); PULLO(wr1, 1, 1, 3); PULLO(wr1, 1, 2, 0); PULLO(wr1, 1, 3, 1); PIN();
    PULLO(wr2, 2, 0, 0); PULLO(wr2, 2, 1, 1); PULLO(wr2, 2, 2, 2); PULLO(wr2, 2, 3, 3); PIN();
    PULLO(wr3, 3, 0, 2); PULLO(wr3, 3, 1, 3); PULLO(wr3, 3, 2, 0); PULLO(wr3, 3, 3, 1); PIN();
    WAITV(1);
    COUNT(vflo, cT, 512);
    if (!has2) {  DIAG(); asm volatile("s_endpgm" ::: "memory"); }
    CST(25);
    PULLO(wr4, 4, 0, 0); PULLO(wr4, 4, 1, 1); PULLO(wr4, 4, 2, 2); PULLO(wr4, 4, 3, 3); PIN();
    PULLO(wr5, 5, 0, 2); PULLO(wr5, 5, 1, 3); PULLO(wr5, 5, 2, 0); PULLO(wr5, 5, 3, 1); PIN();
    PULLO(wr6, 6, 0, 0); PULLO(wr6, 6, 1, 1); PULLO(wr6, 6, 2, 2); PULLO(wr6, 6, 3, 3); PIN();
    PULLO(wr7, 7, 0, 2); PULLO(wr7, 7, 1, 3); PULLO(wr7, 7, 2, 0); PULLO(wr7, 7, 3, 1); PIN();
    WAITR(bb, 1);
    FIN_PREP();
    ARRIVE(7);
    PRIO(0);
    CST(27);
    TCQ(0);
    TCC(8);
    asm volatile("" : "=v"(gl)); if (rg == 0) LD4(gl, sws, vgl, sG2);
    TCQ(24);
    sa = sh + 0;
    LD16(af0[0], sws, vst2, sa, 0);
    LD16(af0[1], sws, vst2, sa, 1024);
    sa = sh + 4096;
    LD16(af0[2], sws, vst2, sa, 0);
    LD16(af0[3], sws, vst2, sa, 1024);
    sa = sh + 8192;
    LD16(af1[0], sws, vst2, sa, 0);
    LD16(af1[1], sws, vst2, sa, 1024);
    sa = sh + 12288;
    LD16(af1[2], sws, vst2, sa, 0);
    LD16(af1[3], sws, vst2, sa, 1024);
    TCC(8);
    CST(28);
    sa = sh + 16384;
    LD16(af2[0], sws, vst2, sa, 0);
    LD16(af2[1], sws, vst2, sa, 1024);
    sa = sh + 20480;
    LD16(af2[2], sws, vst2, sa, 0);
    LD16(af2[3], sws, vst2, sa, 1024);
    PEEK();
    WAITR(gl, 12);
    if (rg == 0) *(LAS float*)(L_OMG + l * 4) = gl;
    WAITV(11);
    MS0(oo[0][0], af0[0], wr0[0]);
    MS0(oo[0][1], af0[0], wr0[1]);
    MS0(oo[0][2], af0[0], wr0[2]);
    MS0(oo[0][3], af0[0], wr0[3]);
    WAITV(10);
    MS0(oo[1][0], af0[1], wr0[0]);
    MS0(oo[1][1], af0[1], wr0[1]);
    MS0(oo[1][2], af0[1], wr0[2]);
    MS0(oo[1][3], af0[1], wr0[3]);
    sa = sh + 24576;
    LD16(af3[0], sws, vst2, sa, 0);
    LD16(af3[1], sws, vst2, sa, 1024);
    WAITV(11);
    MS(oo[0][0], af0[2], wr1[0]);
    MS(oo[0][1], af0[2], wr1[1]);
    MS(oo[0][2], af0[2], wr1[2]);
    MS(oo[0][3], af0[2], wr1[3]);
    WAITV(10);
    MS(oo[1][0], af0[3], wr1[0]);
    MS(oo[1][1], af0[3], wr1[1]);
    MS(oo[1][2], af0[3], wr1[2]);
    MS(oo[1][3], af0[3], wr1[3]);
    sa = sh + 28672;
    LD16(af3[2], sws, vst2, sa, 0);
    LD16(af3[3], sws, vst2, sa, 1024);
    PRIO(0);
    WAITV(11);
    MS(oo[0][0], af1[0], wr2[0]);
    MS(oo[0][1], af1[0], wr2[1]);
    MS(oo[0][2], af1[0], wr2[2]);
    MS(oo[0][3], af1[0], wr2[3]);
    WAITV(10);
    MS(oo[1][0], af1[1], wr2[0]);
    MS(oo[1][1], af1[1], wr2[1]);
    MS(oo[1][2], af1[1], wr2[2]);
    MS(oo[1][3], af1[1], wr2[3]);
    WAITV(9);
    MS(oo[0][0], af1[2], wr3[0]);
    MS(oo[0][1], af1[2], wr3[1]);
    MS(oo[0][2], af1[2], wr3[2]);
    MS(oo[0][3], af1[2], wr3[3]);
    WAITV(8);
    MS(oo[1][0], af1[3], wr3[0]);
    MS(oo[1][1], af1[3], wr3[1]);
    MS(oo[1][2], af1[3], wr3[2]);
    MS(oo[1][3], af1[3], wr3[3]);
    WAITV(7);
    MS(oo[0][0], af2[0], wr4[0]);
    MS(oo[0][1], af2[0], wr4[1]);
    MS(oo[0][2], af2[0], wr4[2]);
    MS(oo[0][3], af2[0], wr4[3]);
    WAITV(6);
    MS(oo[1][0], af2[1], wr4[0]);
    MS(oo[1][1], af2[1], wr4[1]);
    MS(oo[1][2], af2[1], wr4[2]);
    MS(oo[1][3], af2[1], wr4[3]);
    WAITV(5);
    MS(oo[0][0], af2[2], wr5[0]);
    MS(oo[0][1], af2[2], wr5[1]);
    MS(oo[0][2], af2[2], wr5[2]);
    MS(oo[0][3], af2[2], wr5[3]);
    WAITV(4);
    MS(oo[1][0], af2[3], wr5[0]);
    MS(oo[1][1], af2[3], wr5[1]);
    MS(oo[1][2], af2[3], wr5[2]);
    MS(oo[1][3], af2[3], wr5[3]);
    WAITV(3);
    MS(oo[0][0], af3[0], wr6[0]);
    MS(oo[0][1], af3[0], wr6[1]);
    MS(oo[0][2], af3[0], wr6[2]);
    MS(oo[0][3], af3[0], wr6[3]);
    WAITV(2);
    MS(oo[1][0], af3[1], wr6[0]);
    MS(oo[1][1], af3[1], wr6[1]);
    MS(oo[1][2], af3[1], wr6[2]);
    MS(oo[1][3], af3[1], wr6[3]);
    MEET(7, 0xf0);
    SETM0(mrg, 32 * K);
    WAITV(1);
    MS(oo[0][0], af3[2], wr7[0]);
    MS(oo[0][1], af3[2], wr7[1]);
    MS(oo[0][2], af3[2], wr7[2]);
    MS(oo[0][3], af3[2], wr7[3]);
    WAITV(0);
    MS(oo[1][0], af3[3], wr7[0]);
    MS(oo[1][1], af3[3], wr7[1]);
    MS(oo[1][2], af3[3], wr7[2]);
    MS(oo[1][3], af3[3], wr7[3]);
    CST(33);
    asm volatile("s_nop 11");
    DUMP16(oo[0], 32 * K); DUMP16(oo[1], 32 * K + 4096);
    CST(34);
    BAR();
    CST(35);
    FINAL();
    CST(63);
    DIAG();
    asm volatile("s_endpgm" : "=s"(ka), "=s"(kb), "=s"(kc), "=s"(sout)::"memory");
  }
  {
    constexpr int role = 0;
    DECLS();
    u4 bb;
    const u32 mrg = opaque_s(rg * 8192), vst = opaque(l * 16);
    u32 bias;
    PRIO(1);
    const u32 vb1 = opaque(l * 2 + lineA * 128 + (rg & 1) * 512);
    PRE_ADDR();
    asm volatile("" : "=v"(bias));
    ARGS();
    if (!idle) LDU16(bias, sb1, vb1, 0);
    PRIO(0);
    CST(1);
    PRE_LOAD();
    ENTRY();
    if (__builtin_expect(idle, 0)) {
      if (rb < 14) {
        PRE_PREP();
        CST(36);
        WAITV(10);
        CST(37);
        PRE_PHI();
        PRE_READ(0); PRE_JOB(0, 1);
        WAITV(10);
        PRE_READ(1); PRE_JOB(1, 2);
        asm volatile("s_waitcnt vmcnt(8)" : "+v"(pg0), "+v"(pg1)::"memory");
        PRE_READ(2); PRE_JOB(2, 0);
        CST(38);
      }
      BAR();
    }
    SU_1();
    SU_3();
    SU_4();
    SU_5();
    SU_R0();
    if (idle) {
      TAG();
      INITC();
      PRIO(0);
      BAR();
      WAITV(0);
      CST(39);
      WGFLAG(WS_WF + (((c >> 2) * 2 + (rg >> 1)) * 16 + rb) * 128);
      BAR();
      BAR();
      BAR();
      PULL(wr0, 0);
      BAR();
      PULL(wr1, 4096);
      BAR();
      PULL(wr2, 32768);
      BAR();
      { u4 g_;
      M0SD(mrg, 98304, swt, 229376, sw2, vw0, "");
      DMA(sw2, vw1, so, 1024);
      DMA(sw2, vw2, so, 2048);
      DMA(sw2, vw3, so, 3072);
      }
      { u4 g_;
      M0SD(mrg, 102400, swt, 286720, sw2, vw0, "");
      DMA(sw2, vw1, so, 1024);
      DMA(sw2, vw2, so, 2048);
      DMA(sw2, vw3, so, 3072);
      }
      CST(14);
      { u4 g_;
      PULL(wr3, 36864);
      M0SD(mrg, 131072, swt, 344064, sw2, vw0, "");
      DMA(sw2, vw1, so, 1024);
      DMA(sw2, vw2, so, 2048);
      DMA(sw2, vw3, so, 3072);
      PULLED();
      }
      { u4 g_;
      WAITV(8);
      PULL(wr4, 0);
      M0SD(mrg, 135168, swt, 28672, sw2, vw0, "");
      DMA(sw2, vw1, so, 1024);
      DMA(sw2, vw2, so, 2048);
      DMA(sw2, vw3, so, 3072);
      PULLED();
      }
      CST(15);
      { u4 g_;
      WAITV(8);
      PULL(wr5, 4096);
      M0SD(mrg, 98304, swt, 86016, sw2, vw0, "");
      DMA(sw2, vw1, so, 1024);
      DMA(sw2, vw2, so, 2048);
      DMA(sw2, vw3, so, 3072);
      PULLED();
      }
      { u4 g_;
      WAITV(8);
      PULL(wr6, 32768);
      M0SD(mrg, 102400, swt, 143360, sw2, vw0, "");
      DMA(sw2, vw1, so, 1024);
      DMA(sw2, vw2, so, 2048);
      DMA(sw2, vw3, so, 3072);
      PULLED();
      }
      CST(16);
      { u4 g_;
      WAITV(8);
      PULL(wr7, 36864);
      M0SD(mrg, 131072, swt, 200704, sw2, vw0, "");
      DMA(sw2, vw1, so, 1024);
      DMA(sw2, vw2, so, 2048);
      DMA(sw2, vw3, so, 3072);
      PULLED();
      }
      { u4 g_;
      M0SD(mrg, 135168, swt, 258048, sw2, vw0, "");
      DMA(sw2, vw1, so, 1024);
      DMA(sw2, vw2, so, 2048);
      DMA(sw2, vw3, so, 3072);
      }
      CST(17);
      WAITV(0);
      __builtin_amdgcn_s_sleep(40);
    } else {
      b8 fr[2][8]; f4 hh[4][4], hb[4]; b4 lo_;

      SU_W16();
      const u32 sT1 = opaque_s(sT + 4096);

      u32 bia; asm volatile("s_waitcnt vmcnt(0)\n\tv_mov_b32 %0, %1\n\ts_nop 1" : "=v"(bia) : "v"(bias) : "memory");
      if (w == 2) { SETM0(mrg, S4 + 4096); WARMX2(3); WARMW(3); }
      BIAS16(0); BIAS16(1); BIAS16(2); BIAS16(3);
      PRIO(2);
      CST(2);
      asm volatile("s_barrier" ::: "memory");
      CST(3);
      fr[0][4] = asb8(lds16(xs0 + 0));
      lo_ = trr(wd0 + 0);
      fr[0][0] = cat(lo_, trr(wd0 + 512));
      fr[0][5] = asb8(lds16(xs0 + 4096));
      fr[0][6] = asb8(lds16(xs0 + 8192));
      fr[0][7] = asb8(lds16(xs0 + 12288));
      lo_ = trr(wd1 + 0);
      fr[0][1] = cat(lo_, trr(wd1 + 512));
      lo_ = trr(wd2 + 0);
      fr[0][2] = cat(lo_, trr(wd2 + 512));
      lo_ = trr(wd3 + 0);
      fr[0][3] = cat(lo_, trr(wd3 + 512));
      PIN(); MSC(hh[0][0], fr[0][0], fr[0][4], hb[0]); PIN();
      fr[1][4] = asb8(lds16(xs1 + 0));
      PIN(); MSC(hh[0][1], fr[0][0], fr[0][5], hb[0]); PIN();
      lo_ = trr(wd0 + 16384);
      PIN(); MSC(hh[0][2], fr[0][0], fr[0][6], hb[0]); PIN();
      fr[1][0] = cat(lo_, trr(wd0 + 16896));
      PIN(); MSC(hh[0][3], fr[0][0], fr[0][7], hb[0]); PIN();
      fr[1][5] = asb8(lds16(xs1 + 4096));
      PIN(); MSC(hh[1][0], fr[0][1], fr[0][4], hb[1]); PIN();
      fr[1][6] = asb8(lds16(xs1 + 8192));
      PIN(); MSC(hh[1][1], fr[0][1], fr[0][5], hb[1]); PIN();
      fr[1][7] = asb8(lds16(xs1 + 12288));
      PIN(); MSC(hh[1][2], fr[0][1], fr[0][6], hb[1]); PIN();
      lo_ = trr(wd1 + 16384);
      PIN(); MSC(hh[1][3], fr[0][1], fr[0][7], hb[1]); PIN();
      fr[1][1] = cat(lo_, trr(wd1 + 16896));
      PIN(); MSC(hh[2][0], fr[0][2], fr[0][4], hb[2]); PIN();
      lo_ = trr(wd2 + 16384);
      PIN(); MSC(hh[2][1], fr[0][2], fr[0][5], hb[2]); PIN();
      fr[1][2] = cat(lo_, trr(wd2 + 16896));
      PIN(); MSC(hh[2][2], fr[0][2], fr[0][6], hb[2]); PIN();
      lo_ = trr(wd3 + 16384);
      PIN(); MSC(hh[2][3], fr[0][2], fr[0][7], hb[2]); PIN();
      fr[1][3] = cat(lo_, trr(wd3 + 16896));
      PIN(); MSC(hh[3][0], fr[0][3], fr[0][4], hb[3]); PIN();
      PIN(); MSC(hh[3][1], fr[0][3], fr[0][5], hb[3]); PIN();
      PIN(); MSC(hh[3][2], fr[0][3], fr[0][6], hb[3]); PIN();
      PIN(); MSC(hh[3][3], fr[0][3], fr[0][7], hb[3]); PIN();
      PIN(); MS(hh[0][0], fr[1][0], fr[1][4]); PIN();
      PIN(); MS(hh[0][1], fr[1][0], fr[1][5]); PIN();
      CST(55);
      BAR_A();
      CST(4);
      fr[0][4] = asb8(lds16(xs0 + 32768));
      PIN(); MS(hh[0][2], fr[1][0], fr[1][6]); PIN();
      lo_ = trr(wd0 + 32768);
      PIN(); MS(hh[0][3], fr[1][0], fr[1][7]); PIN();
      KEEP("v"(hb[0]), "v"(hb[1]), "v"(hb[2]), "v"(hb[3]));
      fr[0][0] = cat(lo_, trr(wd0 + 33280));
      PIN(); MS(hh[1][0], fr[1][1], fr[1][4]); PIN();
      fr[0][5] = asb8(lds16(xs0 + 36864));
      PIN(); MS(hh[1][1], fr[1][1], fr[1][5]); PIN();
      fr[0][6] = asb8(lds16(xs0 + 40960));
      PIN(); MS(hh[1][2], fr[1][1], fr[1][6]); PIN();
      fr[0][7] = asb8(lds16(xs0 + 45056));
      PIN(); MS(hh[1][3], fr[1][1], fr[1][7]); PIN();
      lo_ = trr(wd1 + 32768);
      PIN(); MS(hh[2][0], fr[1][2], fr[1][4]); PIN();
      fr[0][1] = cat(lo_, trr(wd1 + 33280));
      PIN(); MS(hh[2][1], fr[1][2], fr[1][5]); PIN();
      lo_ = trr(wd2 + 32768);
      PIN(); MS(hh[2][2], fr[1][2], fr[1][6]); PIN();
      fr[0][2] = cat(lo_, trr(wd2 + 33280));
      PIN(); MS(hh[2][3], fr[1][2], fr[1][7]); PIN();
      lo_ = trr(wd3 + 32768);
      PIN(); MS(hh[3][0], fr[1][3], fr[1][4]); PIN();
      fr[0][3] = cat(lo_, trr(wd3 + 33280));
      PIN(); MS(hh[3][1], fr[1][3], fr[1][5]); PIN();
      TAG();
      PIN(); MS(hh[3][2], fr[1][3], fr[1][6]); PIN();
      PIN(); MS(hh[3][3], fr[1][3], fr[1][7]); PIN();
      PIN(); MS(hh[0][0], fr[0][0], fr[0][4]); PIN();
      fr[1][4] = asb8(lds16(xs1 + 32768));
      PIN(); MS(hh[0][1], fr[0][0], fr[0][5]); PIN();
      lo_ = trr(wd0 + 49152);
      PIN(); MS(hh[0][2], fr[0][0], fr[0][6]); PIN();
      fr[1][0] = cat(lo_, trr(wd0 + 49664));
      PIN(); MS(hh[0][3], fr[0][0], fr[0][7]); PIN();
      fr[1][5] = asb8(lds16(xs1 + 36864));
      PIN(); MS(hh[1][0], fr[0][1], fr[0][4]); PIN();
      fr[1][6] = asb8(lds16(xs1 + 40960));
      PIN(); MS(hh[1][1], fr[0][1], fr[0][5]); PIN();
      fr[1][7] = asb8(lds16(xs1 + 45056));
      PIN(); MS(hh[1][2], fr[0][1], fr[0][6]); PIN();
      lo_ = trr(wd1 + 49152);
      PIN(); MS(hh[1][3], fr[0][1], fr[0][7]); PIN();
      fr[1][1] = cat(lo_, trr(wd1 + 49664));
      PIN(); MS(hh[2][0], fr[0][2], fr[0][4]); PIN();
      lo_ = trr(wd2 + 49152);
      PIN(); MS(hh[2][1], fr[0][2], fr[0][5]); PIN();
      fr[1][2] = cat(lo_, trr(wd2 + 49664));
      PIN(); MS(hh[2][2], fr[0][2], fr[0][6]); PIN();
      lo_ = trr(wd3 + 49152);
      PIN(); MS(hh[2][3], fr[0][2], fr[0][7]); PIN();
      fr[1][3] = cat(lo_, trr(wd3 + 49664));
      PIN(); MS(hh[3][0], fr[0][3], fr[0][4]); PIN();
      FLIP(xs0); FLIP(xs1);
      PIN(); MS(hh[3][1], fr[0][3], fr[0][5]); PIN();
      FLIP(wd0);
      PIN(); MS(hh[3][2], fr[0][3], fr[0][6]); PIN();
      FLIP(wd1); FLIP(wd2);
      if (w == 2) { WARM(sw2, 49, 0); WARM(swo, 13, 0); }
      PIN(); MS(hh[3][3], fr[0][3], fr[0][7]); PIN();
      FLIP(wd3);
      PIN(); MS(hh[0][0], fr[1][0], fr[1][4]); PIN();
      PIN(); MS(hh[0][1], fr[1][0], fr[1][5]); PIN();
      CST(56);
      BAR_A();
      CST(5);
      fr[0][4] = asb8(lds16(xs0 + 0));
      PIN(); MS(hh[0][2], fr[1][0], fr[1][6]); PIN();
      lo_ = trr(wd0 + 0);
      PIN(); MS(hh[0][3], fr[1][0], fr[1][7]); PIN();
      fr[0][0] = cat(lo_, trr(wd0 + 512));
      PIN(); MS(hh[1][0], fr[1][1], fr[1][4]); PIN();
      fr[0][5] = asb8(lds16(xs0 + 4096));
      PIN(); MS(hh[1][1], fr[1][1], fr[1][5]); PIN();
      fr[0][6] = asb8(lds16(xs0 + 8192));
      PIN(); MS(hh[1][2], fr[1][1], fr[1][6]); PIN();
      fr[0][7] = asb8(lds16(xs0 + 12288));
      PIN(); MS(hh[1][3], fr[1][1], fr[1][7]); PIN();
      lo_ = trr(wd1 + 0);
      PIN(); MS(hh[2][0], fr[1][2], fr[1][4]); PIN();
      fr[0][1] = cat(lo_, trr(wd1 + 512));
      PIN(); MS(hh[2][1], fr[1][2], fr[1][5]); PIN();
      lo_ = trr(wd2 + 0);
      PIN(); MS(hh[2][2], fr[1][2], fr[1][6]); PIN();
      fr[0][2] = cat(lo_, trr(wd2 + 512));
      PIN(); MS(hh[2][3], fr[1][2], fr[1][7]); PIN();
      lo_ = trr(wd3 + 0);
      PIN(); MS(hh[3][0], fr[1][3], fr[1][4]); PIN();
      fr[0][3] = cat(lo_, trr(wd3 + 512));
      PIN(); MS(hh[3][1], fr[1][3], fr[1][5]); PIN();
      PIN(); MS(hh[3][2], fr[1][3], fr[1][6]); PIN();
      PIN(); MS(hh[3][3], fr[1][3], fr[1][7]); PIN();
      PIN(); MS(hh[0][0], fr[0][0], fr[0][4]); PIN();
      fr[1][4] = asb8(lds16(xs1 + 0));
      PIN(); MS(hh[0][1], fr[0][0], fr[0][5]); PIN();
      lo_ = trr(wd0 + 16384);
      PIN(); MS(hh[0][2], fr[0][0], fr[0][6]); PIN();
      fr[1][0] = cat(lo_, trr(wd0 + 16896));
      PIN(); MS(hh[0][3], fr[0][0], fr[0][7]); PIN();
      fr[1][5] = asb8(lds16(xs1 + 4096));
      PIN(); MS(hh[1][0], fr[0][1], fr[0][4]); PIN();
      fr[1][6] = asb8(lds16(xs1 + 8192));
      PIN(); MS(hh[1][1], fr[0][1], fr[0][5]); PIN();
      fr[1][7] = asb8(lds16(xs1 + 12288));
      PIN(); MS(hh[1][2], fr[0][1], fr[0][6]); PIN();
      lo_ = trr(wd1 + 16384);
      PIN(); MS(hh[1][3], fr[0][1], fr[0][7]); PIN();
      fr[1][1] = cat(lo_, trr(wd1 + 16896));
      PIN(); MS(hh[2][0], fr[0][2], fr[0][4]); PIN();
      lo_ = trr(wd2 + 16384);
      PIN(); MS(hh[2][1], fr[0][2], fr[0][5]); PIN();
      fr[1][2] = cat(lo_, trr(wd2 + 16896));
      PIN(); MS(hh[2][2], fr[0][2], fr[0][6]); PIN();
      lo_ = trr(wd3 + 16384);
      PIN(); MS(hh[2][3], fr[0][2], fr[0][7]); PIN();
      fr[1][3] = cat(lo_, trr(wd3 + 16896));
      PIN(); MS(hh[3][0], fr[0][3], fr[0][4]); PIN();
      FLIP(xs0); FLIP(xs1);
      PIN(); MS(hh[3][1], fr[0][3], fr[0][5]); PIN();
      FLIP(wd0);
      PIN(); MS(hh[3][2], fr[0][3], fr[0][6]); PIN();
      FLIP(wd1); FLIP(wd2);
      PIN(); MS(hh[3][3], fr[0][3], fr[0][7]); PIN();
      FLIP(wd3);
      PIN(); MS(hh[0][0], fr[1][0], fr[1][4]); PIN();
      PIN(); MS(hh[0][1], fr[1][0], fr[1][5]); PIN();
      CST(57);
      BAR_A();
      CST(6);
      fr[0][4] = asb8(lds16(xs0 + 0));
      PIN(); MS(hh[0][2], fr[1][0], fr[1][6]); PIN();
      lo_ = trr(wd0 + 0);
      PIN(); MS(hh[0][3], fr[1][0], fr[1][7]); PIN();
      fr[0][0] = cat(lo_, trr(wd0 + 512));
      PIN(); MS(hh[1][0], fr[1][1], fr[1][4]); PIN();
      fr[0][5] = asb8(lds16(xs0 + 4096));
      PIN(); MS(hh[1][1], fr[1][1], fr[1][5]); PIN();
      fr[0][6] = asb8(lds16(xs0 + 8192));
      PIN(); MS(hh[1][2], fr[1][1], fr[1][6]); PIN();
      fr[0][7] = asb8(lds16(xs0 + 12288));
      PIN(); MS(hh[1][3], fr[1][1], fr[1][7]); PIN();
      lo_ = trr(wd1 + 0);
      PIN(); MS(hh[2][0], fr[1][2], fr[1][4]); PIN();
      fr[0][1] = cat(lo_, trr(wd1 + 512));
      PIN(); MS(hh[2][1], fr[1][2], fr[1][5]); PIN();
      lo_ = trr(wd2 + 0);
      PIN(); MS(hh[2][2], fr[1][2], fr[1][6]); PIN();
      fr[0][2] = cat(lo_, trr(wd2 + 512));
      PIN(); MS(hh[2][3], fr[1][2], fr[1][7]); PIN();
      lo_ = trr(wd3 + 0);
      PIN(); MS(hh[3][0], fr[1][3], fr[1][4]); PIN();
      fr[0][3] = cat(lo_, trr(wd3 + 512));
      PIN(); MS(hh[3][1], fr[1][3], fr[1][5]); PIN();
      PIN(); MS(hh[3][2], fr[1][3], fr[1][6]); PIN();
      PIN(); MS(hh[3][3], fr[1][3], fr[1][7]); PIN();
      PIN(); MS(hh[0][0], fr[0][0], fr[0][4]); PIN();
      fr[1][4] = asb8(lds16(xs1 + 0));
      PIN(); MS(hh[0][1], fr[0][0], fr[0][5]); PIN();
      lo_ = trr(wd0 + 16384);
      PIN(); MS(hh[0][2], fr[0][0], fr[0][6]); PIN();
      fr[1][0] = cat(lo_, trr(wd0 + 16896));
      PIN(); MS(hh[0][3], fr[0][0], fr[0][7]); PIN();
      fr[1][5] = asb8(lds16(xs1 + 4096));
      PIN(); MS(hh[1][0], fr[0][1], fr[0][4]); PIN();
      fr[1][6] = asb8(lds16(xs1 + 8192));
      PIN(); MS(hh[1][1], fr[0][1], fr[0][5]); PIN();
      fr[1][7] = asb8(lds16(xs1 + 12288));
      PIN(); MS(hh[1][2], fr[0][1], fr[0][6]); PIN();
      lo_ = trr(wd1 + 16384);
      PIN(); MS(hh[1][3], fr[0][1], fr[0][7]); PIN();
      fr[1][1] = cat(lo_, trr(wd1 + 16896));
      PIN(); MS(hh[2][0], fr[0][2], fr[0][4]); PIN();
      lo_ = trr(wd2 + 16384);
      PIN(); MS(hh[2][1], fr[0][2], fr[0][5]); PIN();
      fr[1][2] = cat(lo_, trr(wd2 + 16896));
      PIN(); MS(hh[2][2], fr[0][2], fr[0][6]); PIN();
      lo_ = trr(wd3 + 16384);
      PIN(); MS(hh[2][3], fr[0][2], fr[0][7]); PIN();
      fr[1][3] = cat(lo_, trr(wd3 + 16896));
      PIN(); MS(hh[3][0], fr[0][3], fr[0][4]); PIN();
      PIN(); MS(hh[3][1], fr[0][3], fr[0][5]); PIN();
      PIN(); MS(hh[3][2], fr[0][3], fr[0][6]); PIN();
      PIN(); MS(hh[3][3], fr[0][3], fr[0][7]); PIN();
      PIN(); MS(hh[0][0], fr[1][0], fr[1][4]); PIN();
      PIN(); MS(hh[0][1], fr[1][0], fr[1][5]); PIN();
      CST(58);
      BAR_A();
      CST(7);
      fr[0][4] = asb8(lds16(xs0 + 32768));
      PIN(); MS(hh[0][2], fr[1][0], fr[1][6]); PIN();
      lo_ = trr(wd0 + 32768);
      PIN(); MS(hh[0][3], fr[1][0], fr[1][7]); PIN();
      fr[0][0] = cat(lo_, trr(wd0 + 33280));
      PIN(); MS(hh[1][0], fr[1][1], fr[1][4]); PIN();
      fr[0][5] = asb8(lds16(xs0 + 36864));
      PIN(); MS(hh[1][1], fr[1][1], fr[1][5]); PIN();
      fr[0][6] = asb8(lds16(xs0 + 40960));
      PIN(); MS(hh[1][2], fr[1][1], fr[1][6]); PIN();
      fr[0][7] = asb8(lds16(xs0 + 45056));
      PIN(); MS(hh[1][3], fr[1][1], fr[1][7]); PIN();
      lo_ = trr(wd1 + 32768);
      PIN(); MS(hh[2][0], fr[1][2], fr[1][4]); PIN();
      fr[0][1] = cat(lo_, trr(wd1 + 33280));
      PIN(); MS(hh[2][1], fr[1][2], fr[1][5]); PIN();
      lo_ = trr(wd2 + 32768);
      PIN(); MS(hh[2][2], fr[1][2], fr[1][6]); PIN();
      fr[0][2] = cat(lo_, trr(wd2 + 33280));
      PIN(); MS(hh[2][3], fr[1][2], fr[1][7]); PIN();
      lo_ = trr(wd3 + 32768);
      PIN(); MS(hh[3][0], fr[1][3], fr[1][4]); PIN();
      fr[0][3] = cat(lo_, trr(wd3 + 33280));
      PULL1(wr0, 0, 0);
      PIN(); MS(hh[3][1], fr[1][3], fr[1][5]); PIN();
      PULL1(wr0, 0, 1);
      PIN(); MS(hh[3][2], fr[1][3], fr[1][6]); PIN();
      PULL1(wr0, 0, 2);
      PIN(); MS(hh[3][3], fr[1][3], fr[1][7]); PIN();
      PULL1(wr0, 0, 3);
      PIN(); MS(hh[0][0], fr[0][0], fr[0][4]); PIN();
      fr[1][4] = asb8(lds16(xs1 + 32768));
      PIN(); MS(hh[0][1], fr[0][0], fr[0][5]); PIN();
      lo_ = trr(wd0 + 49152);
      PIN(); MS(hh[0][2], fr[0][0], fr[0][6]); PIN();
      fr[1][0] = cat(lo_, trr(wd0 + 49664));
      PIN(); MS(hh[0][3], fr[0][0], fr[0][7]); PIN();
      fr[1][5] = asb8(lds16(xs1 + 36864));
      PIN(); MS(hh[1][0], fr[0][1], fr[0][4]); PIN();
      fr[1][6] = asb8(lds16(xs1 + 40960));
      PIN(); MS(hh[1][1], fr[0][1], fr[0][5]); PIN();
      fr[1][7] = asb8(lds16(xs1 + 45056));
      PIN(); MS(hh[1][2], fr[0][1], fr[0][6]); PIN();
      lo_ = trr(wd1 + 49152);
      PIN(); MS(hh[1][3], fr[0][1], fr[0][7]); PIN();
      fr[1][1] = cat(lo_, trr(wd1 + 49664));
      PIN(); MS(hh[2][0], fr[0][2], fr[0][4]); PIN();
      lo_ = trr(wd2 + 49152);
      PIN(); MS(hh[2][1], fr[0][2], fr[0][5]); PIN();
      fr[1][2] = cat(lo_, trr(wd2 + 49664));
      PIN(); MS(hh[2][2], fr[0][2], fr[0][6]); PIN();
      lo_ = trr(wd3 + 49152);
      PIN(); MS(hh[2][3], fr[0][2], fr[0][7]); PIN();
      fr[1][3] = cat(lo_, trr(wd3 + 49664));
      PIN(); MS(hh[3][0], fr[0][3], fr[0][4]); PIN();
      FLIP(xs0); FLIP(xs1);
      PIN(); MS(hh[3][1], fr[0][3], fr[0][5]); PIN();
      FLIP(wd0);
      PIN(); MS(hh[3][2], fr[0][3], fr[0][6]); PIN();
      FLIP(wd1); FLIP(wd2);
      PIN(); MS(hh[3][3], fr[0][3], fr[0][7]); PIN();
      FLIP(wd3);
      PIN(); MS(hh[0][0], fr[1][0], fr[1][4]); PIN();
      PIN(); MS(hh[0][1], fr[1][0], fr[1][5]); PIN();
      WAITV(0);
      CST(59);
      BAR_A();
      CST(8);
      fr[0][4] = asb8(lds16(xs0 + 0));
      PIN(); MS(hh[0][2], fr[1][0], fr[1][6]); PIN();
      lo_ = trr(wd0 + 0);
      PIN(); MS(hh[0][3], fr[1][0], fr[1][7]); PIN();
      fr[0][0] = cat(lo_, trr(wd0 + 512));
      PIN(); MS(hh[1][0], fr[1][1], fr[1][4]); PIN();
      fr[0][5] = asb8(lds16(xs0 + 4096));
      PIN(); MS(hh[1][1], fr[1][1], fr[1][5]); PIN();
      fr[0][6] = asb8(lds16(xs0 + 8192));
      PIN(); MS(hh[1][2], fr[1][1], fr[1][6]); PIN();
      fr[0][7] = asb8(lds16(xs0 + 12288));
      PIN(); MS(hh[1][3], fr[1][1], fr[1][7]); PIN();
      lo_ = trr(wd1 + 0);
      PIN(); MS(hh[2][0], fr[1][2], fr[1][4]); PIN();
      fr[0][1] = cat(lo_, trr(wd1 + 512));
      PIN(); MS(hh[2][1], fr[1][2], fr[1][5]); PIN();
      lo_ = trr(wd2 + 0);
      PIN(); MS(hh[2][2], fr[1][2], fr[1][6]); PIN();
      fr[0][2] = cat(lo_, trr(wd2 + 512));
      PIN(); MS(hh[2][3], fr[1][2], fr[1][7]); PIN();
      lo_ = trr(wd3 + 0);
      PIN(); MS(hh[3][0], fr[1][3], fr[1][4]); PIN();
      fr[0][3] = cat(lo_, trr(wd3 + 512));
      PULL1(wr1, 4096, 0);
      PIN(); MS(hh[3][1], fr[1][3], fr[1][5]); PIN();
      PULL1(wr1, 4096, 1);
      PIN(); MS(hh[3][2], fr[1][3], fr[1][6]); PIN();
      PULL1(wr1, 4096, 2);
      PIN(); MS(hh[3][3], fr[1][3], fr[1][7]); PIN();
      PULL1(wr1, 4096, 3);
      PIN(); MS(hh[0][0], fr[0][0], fr[0][4]); PIN();
      fr[1][4] = asb8(lds16(xs1 + 0));
      PIN(); MS(hh[0][1], fr[0][0], fr[0][5]); PIN();
      lo_ = trr(wd0 + 16384);
      PIN(); MS(hh[0][2], fr[0][0], fr[0][6]); PIN();
      fr[1][0] = cat(lo_, trr(wd0 + 16896));
      PIN(); MS(hh[0][3], fr[0][0], fr[0][7]); PIN();
      fr[1][5] = asb8(lds16(xs1 + 4096));
      PIN(); MS(hh[1][0], fr[0][1], fr[0][4]); PIN();
      fr[1][6] = asb8(lds16(xs1 + 8192));
      PIN(); MS(hh[1][1], fr[0][1], fr[0][5]); PIN();
      fr[1][7] = asb8(lds16(xs1 + 12288));
      PIN(); MS(hh[1][2], fr[0][1], fr[0][6]); PIN();
      lo_ = trr(wd1 + 16384);
      PIN(); MS(hh[1][3], fr[0][1], fr[0][7]); PIN();
      fr[1][1] = cat(lo_, trr(wd1 + 16896));
      PIN(); MS(hh[2][0], fr[0][2], fr[0][4]); PIN();
      lo_ = trr(wd2 + 16384);
      PIN(); MS(hh[2][1], fr[0][2], fr[0][5]); PIN();
      fr[1][2] = cat(lo_, trr(wd2 + 16896));
      PIN(); MS(hh[2][2], fr[0][2], fr[0][6]); PIN();
      lo_ = trr(wd3 + 16384);
      PIN(); MS(hh[2][3], fr[0][2], fr[0][7]); PIN();
      fr[1][3] = cat(lo_, trr(wd3 + 16896));
      PIN(); MS(hh[3][0], fr[0][3], fr[0][4]); PIN();
      FLIP(xs0); FLIP(xs1);
      PIN(); MS(hh[3][1], fr[0][3], fr[0][5]); PIN();
      FLIP(wd0);
      PIN(); MS(hh[3][2], fr[0][3], fr[0][6]); PIN();
      FLIP(wd1); FLIP(wd2);
      PIN(); MS(hh[3][3], fr[0][3], fr[0][7]); PIN();
      FLIP(wd3);
      PIN(); MS(hh[0][0], fr[1][0], fr[1][4]); PIN();
      PIN(); MS(hh[0][1], fr[1][0], fr[1][5]); PIN();
      CST(60);
      BAR_A();
      CST(9);
      fr[0][4] = asb8(lds16(xs0 + 0));
      PIN(); MS(hh[0][2], fr[1][0], fr[1][6]); PIN();
      lo_ = trr(wd0 + 0);
      PIN(); MS(hh[0][3], fr[1][0], fr[1][7]); PIN();
      fr[0][0] = cat(lo_, trr(wd0 + 512));
      PIN(); MS(hh[1][0], fr[1][1], fr[1][4]); PIN();
      fr[0][5] = asb8(lds16(xs0 + 4096));
      PIN(); MS(hh[1][1], fr[1][1], fr[1][5]); PIN();
      fr[0][6] = asb8(lds16(xs0 + 8192));
      PIN(); MS(hh[1][2], fr[1][1], fr[1][6]); PIN();
      fr[0][7] = asb8(lds16(xs0 + 12288));
      PIN(); MS(hh[1][3], fr[1][1], fr[1][7]); PIN();
      lo_ = trr(wd1 + 0);
      PIN(); MS(hh[2][0], fr[1][2], fr[1][4]); PIN();
      fr[0][1] = cat(lo_, trr(wd1 + 512));
      PIN(); MS(hh[2][1], fr[1][2], fr[1][5]); PIN();
      lo_ = trr(wd2 + 0);
      PIN(); MS(hh[2][2], fr[1][2], fr[1][6]); PIN();
      fr[0][2] = cat(lo_, trr(wd2 + 512));
      PIN(); MS(hh[2][3], fr[1][2], fr[1][7]); PIN();
      lo_ = trr(wd3 + 0);
      PIN(); MS(hh[3][0], fr[1][3], fr[1][4]); PIN();
      fr[0][3] = cat(lo_, trr(wd3 + 512));
      PULL1(wr2, 32768, 0);
      PIN(); MS(hh[3][1], fr[1][3], fr[1][5]); PIN();
      PULL1(wr2, 32768, 1);
      PIN(); MS(hh[3][2], fr[1][3], fr[1][6]); PIN();
      PULL1(wr2, 32768, 2);
      PIN(); MS(hh[3][3], fr[1][3], fr[1][7]); PIN();
      PULL1(wr2, 32768, 3);
      PIN(); MS(hh[0][0], fr[0][0], fr[0][4]); PIN();
      fr[1][4] = asb8(lds16(xs1 + 0));
      PIN(); MS(hh[0][1], fr[0][0], fr[0][5]); PIN();
      lo_ = trr(wd0 + 16384);
      PIN(); MS(hh[0][2], fr[0][0], fr[0][6]); PIN();
      fr[1][0] = cat(lo_, trr(wd0 + 16896));
      PIN(); MS(hh[0][3], fr[0][0], fr[0][7]); PIN();
      fr[1][5] = asb8(lds16(xs1 + 4096));
      PIN(); MS(hh[1][0], fr[0][1], fr[0][4]); PIN();
      fr[1][6] = asb8(lds16(xs1 + 8192));
      PIN(); MS(hh[1][1], fr[0][1], fr[0][5]); PIN();
      fr[1][7] = asb8(lds16(xs1 + 12288));
      PIN(); MS(hh[1][2], fr[0][1], fr[0][6]); PIN();
      lo_ = trr(wd1 + 16384);
      PIN(); MS(hh[1][3], fr[0][1], fr[0][7]); PIN();
      fr[1][1] = cat(lo_, trr(wd1 + 16896));
      PIN(); MS(hh[2][0], fr[0][2], fr[0][4]); PIN();
      lo_ = trr(wd2 + 16384);
      PIN(); MS(hh[2][1], fr[0][2], fr[0][5]); PIN();
      fr[1][2] = cat(lo_, trr(wd2 + 16896));
      PIN(); MS(hh[2][2], fr[0][2], fr[0][6]); PIN();
      lo_ = trr(wd3 + 16384);
      PIN(); MS(hh[2][3], fr[0][2], fr[0][7]); PIN();
      fr[1][3] = cat(lo_, trr(wd3 + 16896));
      PIN(); MS(hh[3][0], fr[0][3], fr[0][4]); PIN();
      PIN(); MS(hh[3][1], fr[0][3], fr[0][5]); PIN();
      PIN(); MS(hh[3][2], fr[0][3], fr[0][6]); PIN();
      PIN(); MS(hh[3][3], fr[0][3], fr[0][7]); PIN();
      PIN(); MS(hh[0][0], fr[1][0], fr[1][4]); PIN();
      PIN(); MS(hh[0][1], fr[1][0], fr[1][5]); PIN();
      CST(61);
      BAR_A();
      CST(10);
      PIN(); MS(hh[0][2], fr[1][0], fr[1][6]); PIN();
      PIN(); MS(hh[0][3], fr[1][0], fr[1][7]); PIN();
      PIN(); MS(hh[1][0], fr[1][1], fr[1][4]); PIN();
      PIN(); MS(hh[1][1], fr[1][1], fr[1][5]); PIN();
      PIN(); MS(hh[1][2], fr[1][1], fr[1][6]); PIN();
      PIN(); MS(hh[1][3], fr[1][1], fr[1][7]); PIN();
      PIN(); MS(hh[2][0], fr[1][2], fr[1][4]); PIN();
      PIN(); MS(hh[2][1], fr[1][2], fr[1][5]); PIN();
      PIN(); MS(hh[2][2], fr[1][2], fr[1][6]); PIN();
      PIN(); MS(hh[2][3], fr[1][2], fr[1][7]); PIN();
      PIN(); MS(hh[3][0], fr[1][3], fr[1][4]); PIN();
      PIN(); MS(hh[3][1], fr[1][3], fr[1][5]); PIN();
      PIN(); MS(hh[3][2], fr[1][3], fr[1][6]); PIN();
      PIN(); MS(hh[3][3], fr[1][3], fr[1][7]); PIN();
      asm volatile("s_nop 7");
      PRIO(0);
      CST(12);
      { u4 g_;
      PRIO(2);
      M0SD(mrg, 98304, swt, 229376, sw2, vw0, "");
      DMA(sw2, vw1, so, 1024);
      GELU4(hh[0][0], 0, g_, 0);
      DMA(sw2, vw2, so, 2048);
      DMA(sw2, vw3, so, 3072);
      GELU4(hh[1][0], 0, g_, 1);
      ST16(sws, vst, sT, 0, g_);
      }
      { u4 g_;
      M0SD(mrg, 102400, swt, 286720, sw2, vw0, "");
      DMA(sw2, vw1, so, 1024);
      GELU4(hh[0][1], 0, g_, 0);
      DMA(sw2, vw2, so, 2048);
      DMA(sw2, vw3, so, 3072);
      GELU4(hh[1][1], 0, g_, 1);
      ST16(sws, vst, sT, 1024, g_);
      }
      CST(14);
      { u4 g_;
      PULL(wr3, 36864);
      M0SD(mrg, 131072, swt, 344064, sw2, vw0, "");
      DMA(sw2, vw1, so, 1024);
      GELU4(hh[0][2], 0, g_, 0);
      DMA(sw2, vw2, so, 2048);
      DMA(sw2, vw3, so, 3072);
      PULLED();
      GELU4(hh[1][2], 0, g_, 1);
      ST16(sws, vst, sT, 2048, g_);
      }
      { u4 g_;
      WAITV(11);
      PULL(wr4, 0);
      M0SD(mrg, 135168, swt, 28672, sw2, vw0, "");
      DMA(sw2, vw1, so, 1024);
      GELU4(hh[0][3], 0, g_, 0);
      DMA(sw2, vw2, so, 2048);
      DMA(sw2, vw3, so, 3072);
      PULLED();
      GELU4(hh[1][3], 0, g_, 1);
      ST16(sws, vst, sT, 3072, g_);
      }
      CST(15);
      { u4 g_;
      PRIO(0);
      WAITV(11);
      PULL(wr5, 4096);
      M0SD(mrg, 98304, swt, 86016, sw2, vw0, "");
      DMA(sw2, vw1, so, 1024);
      GELU4(hh[2][0], 0, g_, 0);
      DMA(sw2, vw2, so, 2048);
      DMA(sw2, vw3, so, 3072);
      PULLED();
      GELU4(hh[3][0], 0, g_, 1);
      ST16(sws, vst, sT1, 0, g_);
      }
      { u4 g_;
      WAITV(11);
      PULL(wr6, 32768);
      M0SD(mrg, 102400, swt, 143360, sw2, vw0, "");
      DMA(sw2, vw1, so, 1024);
      GELU4(hh[2][1], 0, g_, 0);
      CST(22);
      WAITV(7);
      CST(23);
      COUNT(vfl, cT, 0);
      DMA(sw2, vw2, so, 2048);
      DMA(sw2, vw3, so, 3072);
      PULLED();
      GELU4(hh[3][1], 0, g_, 1);
      ST16(sws, vst, sT1, 1024, g_);
      }
      CST(16);
      { u4 g_;
      PULL(wr7, 36864);
      M0SD(mrg, 131072, swt, 200704, sw2, vw0, "");
      DMA(sw2, vw1, so, 1024);
      GELU4(hh[2][2], 0, g_, 0);
      DMA(sw2, vw2, so, 2048);
      DMA(sw2, vw3, so, 3072);
      PULLED();
      GELU4(hh[3][2], 0, g_, 1);
      ST16(sws, vst, sT1, 2048, g_);
      }
      { u4 g_;
      M0SD(mrg, 135168, swt, 258048, sw2, vw0, "");
      DMA(sw2, vw1, so, 1024);
      GELU4(hh[2][3], 0, g_, 0);
      DMA(sw2, vw2, so, 2048);
      DMA(sw2, vw3, so, 3072);
      GELU4(hh[3][3], 0, g_, 1);
      ST16(sws, vst, sT1, 3072, g_);
      }
      CST(17);
    }
    if (!has2) { WAITV(0); COUNT(vfl, cT, 128);  DIAG(); asm volatile("s_endpgm" ::: "memory"); }
    TCQ(0);
    TCC(7);
    CST(20);
    sa = sh + 0;
    LD16(af0[0], sws, vst2, sa, 0);
    LD16(af0[1], sws, vst2, sa, 1024);
    LD16(af0[2], sws, vst2, sa, 2048);
    LD16(af0[3], sws, vst2, sa, 3072);
    sa = sh + 8192;
    LD16(af1[0], sws, vst2, sa, 0);
    LD16(af1[1], sws, vst2, sa, 1024);
    LD16(af1[2], sws, vst2, sa, 2048);
    LD16(af1[3], sws, vst2, sa, 3072);
    CST(24);
    WAITV(8);
    CST(25);
    COUNT(vfl, cT, 128);
    CST(21);
    WAITV(8);
    MS0(oo[0][0], af0[0], wr0[0]); PIN();
    MS0(oo[0][1], af0[0], wr0[1]); PIN();
    MS0(oo[0][2], af0[0], wr0[2]); PIN();
    MS0(oo[0][3], af0[0], wr0[3]); PIN();
    WAITV(7);
    MS0(oo[1][0], af0[1], wr0[0]); PIN();
    MS0(oo[1][1], af0[1], wr0[1]); PIN();
    MS0(oo[1][2], af0[1], wr0[2]); PIN();
    MS0(oo[1][3], af0[1], wr0[3]); PIN();
    WAITV(6);
    MS0(oo[2][0], af0[2], wr0[0]); PIN();
    MS0(oo[2][1], af0[2], wr0[1]); PIN();
    MS0(oo[2][2], af0[2], wr0[2]); PIN();
    MS0(oo[2][3], af0[2], wr0[3]); PIN();
    WAITV(5);
    MS0(oo[3][0], af0[3], wr0[0]); PIN();
    MS0(oo[3][1], af0[3], wr0[1]); PIN();
    MS0(oo[3][2], af0[3], wr0[2]); PIN();
    MS0(oo[3][3], af0[3], wr0[3]); PIN();
    CST(40);
    WAITV(4);
    MS(oo[0][0], af1[0], wr1[0]); PIN();
    sa = sh + 16384;
    LD16(af2[0], sws, vst2, sa, 0);
    MS(oo[0][1], af1[0], wr1[1]); PIN();
    MS(oo[0][2], af1[0], wr1[2]); PIN();
    LD16(af2[1], sws, vst2, sa, 1024);
    MS(oo[0][3], af1[0], wr1[3]); PIN();
    WAITV(5);
    MS(oo[1][0], af1[1], wr1[0]); PIN();
    LD16(af2[2], sws, vst2, sa, 2048);
    MS(oo[1][1], af1[1], wr1[1]); PIN();
    MS(oo[1][2], af1[1], wr1[2]); PIN();
    LD16(af2[3], sws, vst2, sa, 3072);
    MS(oo[1][3], af1[1], wr1[3]); PIN();
    WAITV(6);
    MS(oo[2][0], af1[2], wr1[0]); PIN();
    sa = sh + 24576;
    LD16(af3[0], sws, vst2, sa, 0);
    MS(oo[2][1], af1[2], wr1[1]); PIN();
    MS(oo[2][2], af1[2], wr1[2]); PIN();
    LD16(af3[1], sws, vst2, sa, 1024);
    MS(oo[2][3], af1[2], wr1[3]); PIN();
    WAITV(7);
    MS(oo[3][0], af1[3], wr1[0]); PIN();
    LD16(af3[2], sws, vst2, sa, 2048);
    MS(oo[3][1], af1[3], wr1[1]); PIN();
    MS(oo[3][2], af1[3], wr1[2]); PIN();
    LD16(af3[3], sws, vst2, sa, 3072);
    MS(oo[3][3], af1[3], wr1[3]); PIN();
    CST(41);
    WAITV(7);
    MS(oo[0][0], af2[0], wr2[0]); PIN();
    PULL1(wr0, 0, 1);
    PULL1(wr0, 0, 0);
    sa = sh + 32768;
    LD16(af4[0], sws, vst2, sa, 0);
    MS(oo[0][1], af2[0], wr2[1]); PIN();
    MS(oo[0][2], af2[0], wr2[2]); PIN();
    PULL1(wr0, 0, 3);
    PULL1(wr0, 0, 2);
    LD16(af4[1], sws, vst2, sa, 1024);
    MS(oo[0][3], af2[0], wr2[3]); PIN();
    WAITV(8);
    MS(oo[1][0], af2[1], wr2[0]); PIN();
    LD16(af4[2], sws, vst2, sa, 2048);
    MS(oo[1][1], af2[1], wr2[1]); PIN();
    MS(oo[1][2], af2[1], wr2[2]); PIN();
    LD16(af4[3], sws, vst2, sa, 3072);
    MS(oo[1][3], af2[1], wr2[3]); PIN();
    WAITV(9);
    MS(oo[2][0], af2[2], wr2[0]); PIN();
    PULLED();
    M0SD(mrg, 98304, swt, 315392, sw2, vw0, "");
    MS(oo[2][1], af2[2], wr2[1]); PIN();
    MS(oo[2][2], af2[2], wr2[2]); PIN();
    DMA(sw2, vw1, so, 1024);
    MS(oo[2][3], af2[2], wr2[3]); PIN();
    WAITV(10);
    MS(oo[3][0], af2[3], wr2[0]); PIN();
    DMA(sw2, vw2, so, 2048);
    MS(oo[3][1], af2[3], wr2[1]); PIN();
    MS(oo[3][2], af2[3], wr2[2]); PIN();
    DMA(sw2, vw3, so, 3072);
    MS(oo[3][3], af2[3], wr2[3]); PIN();
    CST(42);
    WAITV(11);
    MS(oo[0][0], af3[0], wr3[0]); PIN();
    PULL1(wr1, 4096, 1);
    PULL1(wr1, 4096, 0);
    sa = sh + 40960;
    LD16(af5[0], sws, vst2, sa, 0);
    MS(oo[0][1], af3[0], wr3[1]); PIN();
    MS(oo[0][2], af3[0], wr3[2]); PIN();
    PULL1(wr1, 4096, 3);
    PULL1(wr1, 4096, 2);
    LD16(af5[1], sws, vst2, sa, 1024);
    MS(oo[0][3], af3[0], wr3[3]); PIN();
    WAITV(12);
    MS(oo[1][0], af3[1], wr3[0]); PIN();
    LD16(af5[2], sws, vst2, sa, 2048);
    MS(oo[1][1], af3[1], wr3[1]); PIN();
    MS(oo[1][2], af3[1], wr3[2]); PIN();
    LD16(af5[3], sws, vst2, sa, 3072);
    MS(oo[1][3], af3[1], wr3[3]); PIN();
    WAITV(13);
    MS(oo[2][0], af3[2], wr3[0]); PIN();
    PULLED();
    M0SD(mrg, 102400, swt, 372736, sw2, vw0, "");
    MS(oo[2][1], af3[2], wr3[1]); PIN();
    MS(oo[2][2], af3[2], wr3[2]); PIN();
    DMA(sw2, vw1, so, 1024);
    MS(oo[2][3], af3[2], wr3[3]); PIN();
    WAITV(14);
    MS(oo[3][0], af3[3], wr3[0]); PIN();
    DMA(sw2, vw2, so, 2048);
    MS(oo[3][1], af3[3], wr3[1]); PIN();
    MS(oo[3][2], af3[3], wr3[2]); PIN();
    DMA(sw2, vw3, so, 3072);
    MS(oo[3][3], af3[3], wr3[3]); PIN();
    CST(43);
    WAITV(15);
    MS(oo[0][0], af4[0], wr4[0]); PIN();
    sa = sh + 49152;
    LD16(af6[0], sws, vst2, sa, 0);
    MS(oo[0][1], af4[0], wr4[1]); PIN();
    MS(oo[0][2], af4[0], wr4[2]); PIN();
    LD16(af6[1], sws, vst2, sa, 1024);
    MS(oo[0][3], af4[0], wr4[3]); PIN();
    WAITV(16);
    MS(oo[1][0], af4[1], wr4[0]); PIN();
    LD16(af6[2], sws, vst2, sa, 2048);
    MS(oo[1][1], af4[1], wr4[1]); PIN();
    MS(oo[1][2], af4[1], wr4[2]); PIN();
    LD16(af6[3], sws, vst2, sa, 3072);
    MS(oo[1][3], af4[1], wr4[3]); PIN();
    WAITV(17);
    MS(oo[2][0], af4[2], wr4[0]); PIN();
    MS(oo[2][1], af4[2], wr4[1]); PIN();
    MS(oo[2][2], af4[2], wr4[2]); PIN();
    TCQ(1);
    MS(oo[2][3], af4[2], wr4[3]); PIN();
    WAITV(16);
    MS(oo[3][0], af4[3], wr4[0]); PIN();
    MS(oo[3][1], af4[3], wr4[1]); PIN();
    MS(oo[3][2], af4[3], wr4[2]); PIN();
    MS(oo[3][3], af4[3], wr4[3]); PIN();
    CST(44);
    WAITV(11);
    MS(oo[0][0], af5[0], wr5[0]); PIN();
    TCC(7);
    sa = sh + 4096;
    LD16(af7[0], sws, vst2, sa, 0);
    MS(oo[0][1], af5[0], wr5[1]); PIN();
    MS(oo[0][2], af5[0], wr5[2]); PIN();
    LD16(af7[1], sws, vst2, sa, 1024);
    MS(oo[0][3], af5[0], wr5[3]); PIN();
    WAITV(12);
    MS(oo[1][0], af5[1], wr5[0]); PIN();
    LD16(af7[2], sws, vst2, sa, 2048);
    MS(oo[1][1], af5[1], wr5[1]); PIN();
    MS(oo[1][2], af5[1], wr5[2]); PIN();
    LD16(af7[3], sws, vst2, sa, 3072);
    MS(oo[1][3], af5[1], wr5[3]); PIN();
    WAITV(13);
    MS(oo[2][0], af5[2], wr5[0]); PIN();
    MS(oo[2][1], af5[2], wr5[1]); PIN();
    MS(oo[2][2], af5[2], wr5[2]); PIN();
    MS(oo[2][3], af5[2], wr5[3]); PIN();
    WAITV(12);
    MS(oo[3][0], af5[3], wr5[0]); PIN();
    MS(oo[3][1], af5[3], wr5[1]); PIN();
    MS(oo[3][2], af5[3], wr5[2]); PIN();
    MS(oo[3][3], af5[3], wr5[3]); PIN();
    CST(45);
    WAITV(7);
    MS(oo[0][0], af6[0], wr6[0]); PIN();
    sa = sh + 12288;
    LD16(af8[0], sws, vst2, sa, 0);
    MS(oo[0][1], af6[0], wr6[1]); PIN();
    MS(oo[0][2], af6[0], wr6[2]); PIN();
    LD16(af8[1], sws, vst2, sa, 1024);
    MS(oo[0][3], af6[0], wr6[3]); PIN();
    WAITV(8);
    MS(oo[1][0], af6[1], wr6[0]); PIN();
    LD16(af8[2], sws, vst2, sa, 2048);
    MS(oo[1][1], af6[1], wr6[1]); PIN();
    MS(oo[1][2], af6[1], wr6[2]); PIN();
    LD16(af8[3], sws, vst2, sa, 3072);
    MS(oo[1][3], af6[1], wr6[3]); PIN();
    WAITV(9);
    MS(oo[2][0], af6[2], wr6[0]); PIN();
    MS(oo[2][1], af6[2], wr6[1]); PIN();
    MS(oo[2][2], af6[2], wr6[2]); PIN();
    MS(oo[2][3], af6[2], wr6[3]); PIN();
    WAITV(8);
    MS(oo[3][0], af6[3], wr6[0]); PIN();
    MS(oo[3][1], af6[3], wr6[1]); PIN();
    MS(oo[3][2], af6[3], wr6[2]); PIN();
    MS(oo[3][3], af6[3], wr6[3]); PIN();
    CST(46);
    WAITV(7);
    MS(oo[0][0], af7[0], wr7[0]); PIN();
    PULL1(wr2, 32768, 1);
    PULL1(wr2, 32768, 0);
    sa = sh + 20480;
    LD16(af9[0], sws, vst2, sa, 0);
    MS(oo[0][1], af7[0], wr7[1]); PIN();
    MS(oo[0][2], af7[0], wr7[2]); PIN();
    PULL1(wr2, 32768, 3);
    PULL1(wr2, 32768, 2);
    LD16(af9[1], sws, vst2, sa, 1024);
    MS(oo[0][3], af7[0], wr7[3]); PIN();
    WAITV(8);
    MS(oo[1][0], af7[1], wr7[0]); PIN();
    LD16(af9[2], sws, vst2, sa, 2048);
    MS(oo[1][1], af7[1], wr7[1]); PIN();
    MS(oo[1][2], af7[1], wr7[2]); PIN();
    LD16(af9[3], sws, vst2, sa, 3072);
    MS(oo[1][3], af7[1], wr7[3]); PIN();
    WAITV(9);
    MS(oo[2][0], af7[2], wr7[0]); PIN();
    MS(oo[2][1], af7[2], wr7[1]); PIN();
    MS(oo[2][2], af7[2], wr7[2]); PIN();
    MS(oo[2][3], af7[2], wr7[3]); PIN();
    WAITV(8);
    MS(oo[3][0], af7[3], wr7[0]); PIN();
    MS(oo[3][1], af7[3], wr7[1]); PIN();
    MS(oo[3][2], af7[3], wr7[2]); PIN();
    MS(oo[3][3], af7[3], wr7[3]); PIN();
    CST(47);
    WAITV(7);
    MS(oo[0][0], af8[0], wr0[0]); PIN();
    PULL1(wr3, 36864, 1);
    PULL1(wr3, 36864, 0);
    sa = sh + 28672;
    LD16(af10[0], sws, vst2, sa, 0);
    MS(oo[0][1], af8[0], wr0[1]); PIN();
    MS(oo[0][2], af8[0], wr0[2]); PIN();
    PULL1(wr3, 36864, 3);
    PULL1(wr3, 36864, 2);
    LD16(af10[1], sws, vst2, sa, 1024);
    MS(oo[0][3], af8[0], wr0[3]); PIN();
    WAITV(8);
    MS(oo[1][0], af8[1], wr0[0]); PIN();
    LD16(af10[2], sws, vst2, sa, 2048);
    MS(oo[1][1], af8[1], wr0[1]); PIN();
    MS(oo[1][2], af8[1], wr0[2]); PIN();
    LD16(af10[3], sws, vst2, sa, 3072);
    MS(oo[1][3], af8[1], wr0[3]); PIN();
    WAITV(9);
    MS(oo[2][0], af8[2], wr0[0]); PIN();
    MS(oo[2][1], af8[2], wr0[1]); PIN();
    MS(oo[2][2], af8[2], wr0[2]); PIN();
    MS(oo[2][3], af8[2], wr0[3]); PIN();
    WAITV(8);
    MS(oo[3][0], af8[3], wr0[0]); PIN();
    MS(oo[3][1], af8[3], wr0[1]); PIN();
    MS(oo[3][2], af8[3], wr0[2]); PIN();
    LD16P(bb, sb2, vcol, 0);
    MS(oo[3][3], af8[3], wr0[3]); PIN();
    CST(48);
    WAITV(8);
    MS(oo[0][0], af9[0], wr1[0]); PIN();
    PULL1(wr4, 0, 1);
    PULL1(wr4, 0, 0);
    sa = sh + 36864;
    LD16(af11[0], sws, vst2, sa, 0);
    MS(oo[0][1], af9[0], wr1[1]); PIN();
    MS(oo[0][2], af9[0], wr1[2]); PIN();
    PULL1(wr4, 0, 3);
    PULL1(wr4, 0, 2);
    LD16(af11[1], sws, vst2, sa, 1024);
    MS(oo[0][3], af9[0], wr1[3]); PIN();
    WAITV(9);
    MS(oo[1][0], af9[1], wr1[0]); PIN();
    LD16(af11[2], sws, vst2, sa, 2048);
    MS(oo[1][1], af9[1], wr1[1]); PIN();
    MS(oo[1][2], af9[1], wr1[2]); PIN();
    LD16(af11[3], sws, vst2, sa, 3072);
    MS(oo[1][3], af9[1], wr1[3]); PIN();
    WAITV(10);
    MS(oo[2][0], af9[2], wr1[0]); PIN();
    MS(oo[2][1], af9[2], wr1[1]); PIN();
    MS(oo[2][2], af9[2], wr1[2]); PIN();
    MS(oo[2][3], af9[2], wr1[3]); PIN();
    WAITV(9);
    MS(oo[3][0], af9[3], wr1[0]); PIN();
    MS(oo[3][1], af9[3], wr1[1]); PIN();
    MS(oo[3][2], af9[3], wr1[2]); PIN();
    MS(oo[3][3], af9[3], wr1[3]); PIN();
    CST(49);
    WAITV(8);
    MS(oo[0][0], af10[0], wr2[0]); PIN();
    PULL1(wr5, 4096, 1);
    PULL1(wr5, 4096, 0);
    sa = sh + 45056;
    LD16(af12[0], sws, vst2, sa, 0);
    MS(oo[0][1], af10[0], wr2[1]); PIN();
    MS(oo[0][2], af10[0], wr2[2]); PIN();
    PULL1(wr5, 4096, 3);
    PULL1(wr5, 4096, 2);
    LD16(af12[1], sws, vst2, sa, 1024);
    MS(oo[0][3], af10[0], wr2[3]); PIN();
    WAITV(9);
    MS(oo[1][0], af10[1], wr2[0]); PIN();
    LD16(af12[2], sws, vst2, sa, 2048);
    MS(oo[1][1], af10[1], wr2[1]); PIN();
    MS(oo[1][2], af10[1], wr2[2]); PIN();
    LD16(af12[3], sws, vst2, sa, 3072);
    MS(oo[1][3], af10[1], wr2[3]); PIN();
    WAITV(10);
    MS(oo[2][0], af10[2], wr2[0]); PIN();
    MS(oo[2][1], af10[2], wr2[1]); PIN();
    MS(oo[2][2], af10[2], wr2[2]); PIN();
    MS(oo[2][3], af10[2], wr2[3]); PIN();
    WAITV(9);
    MS(oo[3][0], af10[3], wr2[0]); PIN();
    MS(oo[3][1], af10[3], wr2[1]); PIN();
    MS(oo[3][2], af10[3], wr2[2]); PIN();
    MS(oo[3][3], af10[3], wr2[3]); PIN();
    CST(50);
    WAITV(7);
    MS(oo[0][0], af11[0], wr3[0]); PIN();
    sa = sh + 53248;
    LD16(af13[0], sws, vst2, sa, 0);
    MS(oo[0][1], af11[0], wr3[1]); PIN();
    MS(oo[0][2], af11[0], wr3[2]); PIN();
    LD16(af13[1], sws, vst2, sa, 1024);
    MS(oo[0][3], af11[0], wr3[3]); PIN();
    WAITV(8);
    MS(oo[1][0], af11[1], wr3[0]); PIN();
    LD16(af13[2], sws, vst2, sa, 2048);
    MS(oo[1][1], af11[1], wr3[1]); PIN();
    MS(oo[1][2], af11[1], wr3[2]); PIN();
    LD16(af13[3], sws, vst2, sa, 3072);
    MS(oo[1][3], af11[1], wr3[3]); PIN();
    WAITV(9);
    MS(oo[2][0], af11[2], wr3[0]); PIN();
    MS(oo[2][1], af11[2], wr3[1]); PIN();
    MS(oo[2][2], af11[2], wr3[2]); PIN();
    MS(oo[2][3], af11[2], wr3[3]); PIN();
    WAITV(8);
    MS(oo[3][0], af11[3], wr3[0]); PIN();
    MS(oo[3][1], af11[3], wr3[1]); PIN();
    MS(oo[3][2], af11[3], wr3[2]); PIN();
    MS(oo[3][3], af11[3], wr3[3]); PIN();
    CST(51);
    WAITV(7);
    MS(oo[0][0], af12[0], wr4[0]); PIN();
    MS(oo[0][1], af12[0], wr4[1]); PIN();
    MS(oo[0][2], af12[0], wr4[2]); PIN();
    MS(oo[0][3], af12[0], wr4[3]); PIN();
    WAITV(6);
    MS(oo[1][0], af12[1], wr4[0]); PIN();
    MS(oo[1][1], af12[1], wr4[1]); PIN();
    MS(oo[1][2], af12[1], wr4[2]); PIN();
    MS(oo[1][3], af12[1], wr4[3]); PIN();
    WAITV(5);
    MS(oo[2][0], af12[2], wr4[0]); PIN();
    MS(oo[2][1], af12[2], wr4[1]); PIN();
    MS(oo[2][2], af12[2], wr4[2]); PIN();
    MS(oo[2][3], af12[2], wr4[3]); PIN();
    WAITV(4);
    MS(oo[3][0], af12[3], wr4[0]); PIN();
    MS(oo[3][1], af12[3], wr4[1]); PIN();
    MS(oo[3][2], af12[3], wr4[2]); PIN();
    MS(oo[3][3], af12[3], wr4[3]); PIN();
    asm volatile("" : "+v"(bb));
    FIN_PREP();
    CST(52);
    PULLED();
    const u32 da = opaque(S4 + mrg + l * 4);
    SETM0(mrg, 40 * K - 128);
    WAITV(3);
    MS(oo[0][0], af13[0], wr5[0]); PIN();
    MS(oo[0][1], af13[0], wr5[1]); PIN();
    MS(oo[0][2], af13[0], wr5[2]); PIN();
    MS(oo[0][3], af13[0], wr5[3]); PIN();
    WAITV(2);
    MS(oo[1][0], af13[1], wr5[0]); PIN();
    MS(oo[1][1], af13[1], wr5[1]); PIN();
    MS(oo[1][2], af13[1], wr5[2]); PIN();
    MS(oo[1][3], af13[1], wr5[3]); PIN();
    WAITV(1);
    MS(oo[2][0], af13[2], wr5[0]); PIN();
    DUMP2(oo[0][0], da, 0);
    DUMP2(oo[0][1], da, 4);
    MS(oo[2][1], af13[2], wr5[1]); PIN();
    MS(oo[2][2], af13[2], wr5[2]); PIN();
    DUMP2(oo[0][2], da, 8);
    DUMP2(oo[0][3], da, 12);
    MS(oo[2][3], af13[2], wr5[3]); PIN();
    WAITV(0);
    MS(oo[3][0], af13[3], wr5[0]); PIN();
    DUMP2(oo[1][0], da, 16);
    DUMP2(oo[1][1], da, 20);
    MS(oo[3][1], af13[3], wr5[1]); PIN();
    MS(oo[3][2], af13[3], wr5[2]); PIN();
    DUMP2(oo[1][2], da, 24);
    DUMP2(oo[1][3], da, 28);
    MS(oo[3][3], af13[3], wr5[3]); PIN();
    CST(53);
    DUMP16(oo[2], 56 * K + 128); DUMP16(oo[3], 60 * K + 128);
    CST(34);
    BAR();
    CST(35);
    FINAL();
    if (bid == 0 && w == 0) WGFLAG(0);
    asm volatile(".Lgm_end:");
    CST(63);
    DIAG();
  }

}

extern "C" void kernel_launch(void* const* d_in, const int* in_sizes, int n_in, void* d_out, int out_size, void* d_ws, size_t ws_size,
                              hipStream_t stream) {
  constexpr u32 F = 0x00020000u;
  const Args a = {{d_in[0], 2048 * 896, F}, {d_in[5], 448 * 3584 + 3072, F}, {d_ws, HT ? 64u << 20 : WS_END, F}, {d_in[6], 3584, F}, {d_in[1], 448 * 2688, F}, {d_in[2], 16 * 1568, F},
                  {d_in[9], 896, F}, {d_in[7], 1792 * 896, F}, {d_in[3], 448 * 896, F}, {d_in[8], 896, F}, {d_out, 2048 * 896, F}};
  gated_mix_kernel<<<256, 512, 0, stream>>>(a);
}
```
